```python
import torch

def _softmax(x, axis=-1):
    return torch.softmax(x, dim=axis)
def _gelu(x):
    return torch.nn.functional.gelu(x, approximate="tanh")
def _silu(x):
    return torch.nn.functional.silu(x)
def _sigmoid(x):
    return torch.sigmoid(x)
def _layernorm(x, axis=-1, eps=1e-5):
    m = x.mean(dim=axis, keepdim=True)
    v = x.var(dim=axis, keepdim=True, unbiased=False)
    return (x - m) / torch.sqrt(v + eps)
def _scatter_add(tgt, idx, src, axis=0):
    return tgt.clone().index_add_(axis, idx, src)
def _segment_sum(src, seg, n):
    out = torch.zeros((n,) + src.shape[1:], dtype=src.dtype)
    return out.index_add_(0, seg, src)
def _pad(x, pw):
    flat = tuple(v for pair in reversed(pw) for v in pair)
    return torch.nn.functional.pad(x, flat)

kernel_name = 'concat_heads__ssm_conv_h3__softmax_full_mha__prompt'

def setup_inputs():

    _w = (lambda a: (lambda t: t.to(torch.bfloat16) if t.is_floating_point() else t)(torch.as_tensor(a)))
    def _A_setup():
        B, S, D = 1, 2048, 1024
        ks = 4
        return {
            "q": _w((lambda *s: torch.randn(*s, dtype=torch.bfloat16) * 0.05)(B, S, D)),
            "k": _w((lambda *s: torch.randn(*s, dtype=torch.bfloat16) * 0.8)(B, S, D)),
            "v": _w((lambda *s: torch.randn(*s, dtype=torch.bfloat16) * 0.8)(B, S, D)),
            "shift_k": _w((lambda *s: torch.randn(*s, dtype=torch.bfloat16) * 0.8)(D, ks) * 0.5),
            "ssm_k": _w((lambda *s: torch.randn(*s, dtype=torch.bfloat16) * 0.8)(D, S) * (1.0 / S ** 0.5)),
            "ssm_D": _w((lambda *s: torch.randn(*s, dtype=torch.bfloat16) * 0.8)(D)),
        }
    _A_in = _A_setup()

    def _B_setup():
        B, S, D = 1, 2048, 1024
        return {
            "x": _w((lambda *s: torch.randn(*s, dtype=torch.bfloat16) * 0.2)(B, S, D)),
            "wqkv": _w((lambda *s: torch.randn(*s, dtype=torch.bfloat16) * 0.2)(D, 3 * D)),
            "wo": _w((lambda *s: torch.randn(*s, dtype=torch.bfloat16) * 0.05)(D, D)),
        }
    _B_in = _B_setup()

    return {
        "A_q": _A_in["q"],
        "A_k": _A_in["k"],
        "A_v": _A_in["v"],
        "A_shift_k": _A_in["shift_k"],
        "A_ssm_k": _A_in["ssm_k"],
        "A_ssm_D": _A_in["ssm_D"],
        "B_x": _B_in["x"],
        "B_wqkv": _B_in["wqkv"],
        "B_wo": _B_in["wo"],
    }

def reference(A_q, A_k, A_v, A_shift_k, A_ssm_k, A_ssm_D, B_x, B_wqkv, B_wo):
    def _A_ref(q, k, v, shift_k, ssm_k, ssm_D):
        B, S, D = q.shape
        ks = shift_k.shape[-1]
        k_pad = _pad(k, ((0, 0), (ks - 1, 0), (0, 0)))
        k_shift = torch.zeros_like(k)
        for t in range(ks):
            k_shift = k_shift + k_pad[:, t:t + S, :] * shift_k[:, ks - 1 - t][None, None, :]
        kv = k_shift * v
        L = 2 * S
        kv_pad = _pad(kv, ((0, 0), (0, S), (0, 0)))
        kf_pad = _pad(ssm_k, ((0, 0), (0, S)))
        kv_f = (lambda x, axes=(-2,-1): torch.fft.rfft2(x.float(), dim=axes))(kv_pad[:, None, :, :], axes=(1, 2))
        kf_f = (lambda x, axes=(-2,-1): torch.fft.rfft2(x.float(), dim=axes))(kf_pad[:, None, :], axes=(1, 2))
        kf_f = (lambda x, *d: x.permute(*d))(kf_f, 1, 2, 0)
        y_f = kv_f * kf_f[None, :, :, :]
        y = (lambda x, s, axes=(-2,-1): torch.fft.irfft2(x, s=s, dim=axes).to(torch.bfloat16))(y_f, (1, L), axes=(1, 2))[:, 0, :S, :]
        y = y + kv * ssm_D[None, None, :]
        return q * y

    def _B_ref(x, wqkv, wo):
        B, S, D = x.shape
        H = 32; Dh = D // H
        qkv = (lambda x, *d: x.permute(*d))((x @ wqkv).reshape(B, S, 3, H, Dh), 2, 0, 3, 1, 4)
        q, k, v = qkv[0], qkv[1], qkv[2]
        scale = Dh ** -0.5
        scores = torch.einsum("bhqd,bhkd->bhqk", q, k) * scale
        p = _softmax(scores, axis=-1)
        o = torch.einsum("bhqk,bhkd->bhqd", p, v)
        return (lambda x, *d: x.permute(*d))(o, 0, 2, 1, 3).reshape(B, S, D) @ wo

    a = _A_ref(A_q, A_k, A_v, A_shift_k, A_ssm_k, A_ssm_D)
    b = _B_ref(B_x, B_wqkv, B_wo)
    return torch.concatenate([a, b], axis=-1)
```

```cpp
#include <hip/hip_runtime.h>
#include <hip/hip_bf16.h>
#include <stdint.h>

typedef short bf16x8 __attribute__((ext_vector_type(8)));
typedef short s16x4 __attribute__((ext_vector_type(4)));
typedef float f32x16 __attribute__((ext_vector_type(16)));
typedef float f32x4 __attribute__((ext_vector_type(4)));
typedef unsigned u32x4 __attribute__((ext_vector_type(4)));
typedef unsigned u32x2 __attribute__((ext_vector_type(2)));
typedef __bf16 bf16v2 __attribute__((ext_vector_type(2)));
typedef float f32x2 __attribute__((ext_vector_type(2)));
#define LAS __attribute__((address_space(3)))
#define DI __device__ __forceinline__
#define MFMA32(a, b, c) __builtin_amdgcn_mfma_f32_32x32x16_bf16((a), (b), (c), 0, 0, 0)
#define MFMA16(a, b, c) __builtin_amdgcn_mfma_f32_16x16x32_bf16((a), (b), (c), 0, 0, 0)

constexpr int S_ = 2048, D_ = 1024, NH = 32;
constexpr size_t WS_Q = 0;
constexpr size_t WS_KV = WS_Q + (4u << 20);
constexpr size_t WS_O = WS_KV + (8u << 20);
constexpr size_t WS_UR = WS_O + (4u << 20);
constexpr size_t WS_KMAX = WS_UR + (4u << 20);

DI unsigned pk2(float lo, float hi) {
  f32x2 v = {lo, hi};
  bf16v2 b = __builtin_convertvector(v, bf16v2);
  return __builtin_bit_cast(unsigned, b);
}
DI float bflo(unsigned u) { return __uint_as_float(u << 16); }
DI float bfhi(unsigned u) { return __uint_as_float(u & 0xffff0000u); }
DI float rbf(float x) { return bflo(pk2(x, x)); }
DI bf16x8 pack8(const f32x16& x, int s, float scale) {
  u32x4 p;
  p[0] = pk2(x[8 * s + 0] * scale, x[8 * s + 1] * scale);
  p[1] = pk2(x[8 * s + 2] * scale, x[8 * s + 3] * scale);
  p[2] = pk2(x[8 * s + 4] * scale, x[8 * s + 5] * scale);
  p[3] = pk2(x[8 * s + 6] * scale, x[8 * s + 7] * scale);
  return __builtin_bit_cast(bf16x8, p);
}
DI bf16x8 pack8n(const f32x16& x, int s) {
  u32x4 p;
  p[0] = pk2(x[8 * s + 0], x[8 * s + 1]);
  p[1] = pk2(x[8 * s + 2], x[8 * s + 3]);
  p[2] = pk2(x[8 * s + 4], x[8 * s + 5]);
  p[3] = pk2(x[8 * s + 6], x[8 * s + 7]);
  return __builtin_bit_cast(bf16x8, p);
}
DI void glds16(const void* gbase, unsigned voff, unsigned lds_dst) {
  unsigned keep;
  asm volatile("s_mov_b32 %0, m0\n\ts_mov_b32 m0, %3\n\ts_nop 0\n\tglobal_load_lds_dwordx4 %1, %2\n\ts_mov_b32 m0, %0"
               : "=&s"(keep) : "v"(voff), "s"(gbase), "s"(lds_dst) : "memory");
}
DI bf16x8 lds_rd128(unsigned a) { return *(const LAS bf16x8*)(uintptr_t)a; }
DI s16x4 lds_tr(unsigned a) {
  typedef short v4i16_t __attribute__((ext_vector_type(4)));
  return __builtin_amdgcn_ds_read_tr16_b64_v4i16((LAS v4i16_t*)(uintptr_t)a);
}
DI bf16x8 cat44(s16x4 lo, s16x4 hi) { return __builtin_shufflevector(lo, hi, 0, 1, 2, 3, 4, 5, 6, 7); }
#define WAITV(n) asm volatile("s_waitcnt vmcnt(" #n ")" ::: "memory")
#define BARRIER_L() asm volatile("s_waitcnt lgkmcnt(0)\n\ts_barrier" ::: "memory")
#define BARRIER() asm volatile("s_barrier" ::: "memory")
DI float xhalf_sum(float v) {
  auto r = __builtin_amdgcn_permlane32_swap(__float_as_uint(v), __float_as_uint(v), false, false);
  return __uint_as_float(r[0]) + __uint_as_float(r[1]);
}
DI float xhalf_max(float v) {
  auto r = __builtin_amdgcn_permlane32_swap(__float_as_uint(v), __float_as_uint(v), false, false);
  return fmaxf(__uint_as_float(r[0]), __uint_as_float(r[1]));
}

constexpr int K1_STAGE = 40960, K1_LDS = 3 * K1_STAGE;
constexpr float QSCALE = 0.17677669529663687f * 1.4426950408889634f;

struct Frags { bf16x8 xf[2]; bf16x8 wf[3]; };

__global__ __launch_bounds__(512, 2) void k1_qkv(const unsigned short* x, const unsigned short* w,
                                                 const unsigned short* ak, const unsigned short* av,
                                                 const unsigned short* shk, char* ws) {
  extern __shared__ __attribute__((aligned(16))) char lds[];
  const unsigned lds0 = (unsigned)(uintptr_t)(LAS char*)lds;
  const int tid = threadIdx.x, lane = tid & 63;
  const int wid = __builtin_amdgcn_readfirstlane(tid >> 6);
  const int bx = blockIdx.x, xg = bx & 7, li = bx >> 3;
  const int mt = 8 * (xg & 1) + (li & 7), nt = 4 * (xg >> 1) + (li >> 3);
  if (wid >= 4) {
    const int lw = wid - 4;
    const unsigned rowl = lane >> 3, sl = lane & 7;
    const unsigned vx0 = (rowl * 1024 + ((sl ^ ((lane >> 4) & 7)) * 8)) * 2;
    const unsigned vx1 = (rowl * 1024 + ((sl ^ ((4 + (lane >> 4)) & 7)) * 8)) * 2;
    const unsigned vw = (rowl * 3072 + ((sl ^ (((lane >> 4) & 1) << 2)) * 8)) * 2;
    const char* xb = (const char*)(x + (size_t)(mt * 128 + lw * 32) * 1024);
    const char* wb = (const char*)(w + nt * 64);
    auto issue = [&](int s, int buf) {
      const unsigned dst = lds0 + buf * K1_STAGE;
      const char* xs = xb + s * 128;
#pragma unroll
      for (int i = 0; i < 4; ++i) glds16(xs + i * 8 * 2048, (i & 1) ? vx1 : vx0, dst + (lw * 4 + i) * 1024);
      const char* wsrc = wb + (size_t)s * 64 * 6144;
#pragma unroll
      for (int i = 0; i < 6; ++i) {
        const int q = lw * 6 + i, sec = q >> 3, p = q & 7;
        glds16(wsrc + (size_t)p * 8 * 6144 + sec * 2048, vw, dst + 16384 + q * 1024);
      }
    };
    issue(0, 0); issue(1, 1); issue(2, 2);
    WAITV(20); BARRIER();
    int buf = 0;
    for (int s = 0; s < 13; ++s) {
      WAITV(10); BARRIER();
      issue(s + 3, buf);
      buf = (buf == 2) ? 0 : buf + 1;
    }
    WAITV(10); BARRIER();
    WAITV(0); BARRIER();
    BARRIER();
    {
      const int lt = tid - 256;
      const int cg = lt & 7, tg = lt >> 3;
      const int ch0 = (bx & 15) * 64 + cg * 8, t0 = (bx >> 4) * 128 + tg * 4;
      u32x4 kr[7], vr[4], wk[4];
#pragma unroll
      for (int r = 0; r < 7; ++r) {
        const int t = t0 - 3 + r;
        kr[r] = (u32x4){0u, 0u, 0u, 0u};
        if (t >= 0) kr[r] = *(const u32x4*)(ak + (size_t)t * 1024 + ch0);
      }
#pragma unroll
      for (int r = 0; r < 4; ++r) vr[r] = *(const u32x4*)(av + (size_t)(t0 + r) * 1024 + ch0);
#pragma unroll
      for (int r = 0; r < 4; ++r) wk[r] = *(const u32x4*)(shk + (size_t)ch0 * 4 + r * 8);
      unsigned short* ur = (unsigned short*)(ws + WS_UR);
#pragma unroll
      for (int c = 0; c < 8; ++c) {
        const unsigned wa = wk[c >> 1][(c & 1) * 2], wb2 = wk[c >> 1][(c & 1) * 2 + 1];
        const float w0 = bflo(wa), w1 = bfhi(wa), w2 = bflo(wb2), w3 = bfhi(wb2);
        float kf[7], u[4];
#pragma unroll
        for (int r = 0; r < 7; ++r) kf[r] = (c & 1) ? bfhi(kr[r][c >> 1]) : bflo(kr[r][c >> 1]);
#pragma unroll
        for (int r = 0; r < 4; ++r) {
          float a = rbf(kf[r] * w3);
          a = rbf(a + rbf(kf[r + 1] * w2));
          a = rbf(a + rbf(kf[r + 2] * w1));
          a = rbf(a + rbf(kf[r + 3] * w0));
          const float vv = (c & 1) ? bfhi(vr[r][c >> 1]) : bflo(vr[r][c >> 1]);
          u[r] = a * vv;
        }
        u32x2 o = {pk2(u[3], u[2]), pk2(u[1], u[0])};
        *(u32x2*)(ur + (size_t)(ch0 + c) * 2048 + (2044 - t0)) = o;
      }
    }
    return;
  }
  const int wm = wid & 1, wn = wid >> 1;
  const int r32 = lane & 31, hi = lane >> 5;
  unsigned xa[4];
#pragma unroll
  for (int kk = 0; kk < 4; ++kk) xa[kk] = lds0 + (wm * 64 + r32) * 128 + (((2 * kk + hi) ^ ((r32 >> 1) & 7)) * 16);
  const int q4 = (lane & 15) >> 2;
  const unsigned wa = lds0 + 16384 + (hi * 8 + q4) * 128 + ((wn ^ (q4 >> 1)) * 64) + ((lane >> 4) & 1) * 32 + (lane & 3) * 8;
  f32x16 acc[2][3];
#pragma unroll
  for (int i = 0; i < 2; ++i)
#pragma unroll
    for (int j = 0; j < 3; ++j) acc[i][j] = (f32x16){};
  auto ld = [&](Frags& f, unsigned so, int kk) {
    f.xf[0] = lds_rd128(xa[kk] + so);
    f.xf[1] = lds_rd128(xa[kk] + so + 4096);
#pragma unroll
    for (int j = 0; j < 3; ++j)
      f.wf[j] = cat44(lds_tr(wa + so + j * 8192 + kk * 2048), lds_tr(wa + so + j * 8192 + kk * 2048 + 512));
  };
  auto mma = [&](const Frags& f) {
#pragma unroll
    for (int i = 0; i < 2; ++i) {
      acc[i][0] = MFMA32(f.wf[0], f.xf[i], acc[i][0]);
      acc[i][1] = MFMA32(f.wf[1], f.xf[i], acc[i][1]);
      acc[i][2] = MFMA32(f.xf[i], f.wf[2], acc[i][2]);
    }
  };
  Frags f0, f1;
  BARRIER();
  unsigned so = 0;
  ld(f0, so, 0);
  for (int s = 0; s < 16; ++s) {
    ld(f1, so, 1); mma(f0);
    ld(f0, so, 2); mma(f1);
    ld(f1, so, 3); mma(f0);
    BARRIER_L();
    so = (so == 2 * K1_STAGE) ? 0 : so + K1_STAGE;
    if (s < 15) ld(f0, so, 0);
    mma(f1);
  }
  const int head = nt * 2 + wn;
  float kn = 0.f;
#pragma unroll
  for (int i = 0; i < 2; ++i) {
    const int tb = mt * 4 + wm * 2 + i;
    char* qd = ws + WS_Q + ((size_t)(head * 64 + tb) * 2) * 1024 + lane * 16;
    char* kvd = ws + WS_KV + ((size_t)(head * 64 + tb) * 4) * 1024 + lane * 16;
    float ss = 0.f;
#pragma unroll
    for (int s = 0; s < 2; ++s) {
      *(bf16x8*)(qd + s * 1024) = pack8(acc[i][0], s, QSCALE);
      const bf16x8 kp = pack8n(acc[i][1], s);
      *(bf16x8*)(kvd + s * 1024) = kp;
      const u32x4 ku = __builtin_bit_cast(u32x4, kp);
#pragma unroll
      for (int e = 0; e < 4; ++e) { const float a = bflo(ku[e]), b = bfhi(ku[e]); ss += a * a + b * b; }
      *(bf16x8*)(kvd + (2 + s) * 1024) = pack8n(acc[i][2], s);
    }
    kn = fmaxf(kn, xhalf_sum(ss));
  }
#pragma unroll
  for (int o = 1; o < 32; o <<= 1) kn = fmaxf(kn, __shfl_xor(kn, o));
  if (lane == 0) ((float*)(ws + WS_KMAX))[head * 32 + mt * 2 + wm] = kn;
}

constexpr int K2_STAGE = 16384, K2_NST = 3;
constexpr int CV_URE = 0, CV_URO = 4160, CV_HP = 8320, CV_Z = 12928, CV_SZ = 17024;
constexpr int K2_CV = K2_NST * K2_STAGE, K2_LDS = K2_CV + 4 * CV_SZ;

DI void attn_tile(const bf16x8 k0, const bf16x8 k1, const bf16x8 v0, const bf16x8 v1, const bf16x8 q0, const bf16x8 q1,
                  const f32x16& negm, const bf16x8 ones, f32x16& o, f32x4& l) {
  f32x16 s = MFMA32(k0, q0, negm);
  s = MFMA32(k1, q1, s);
#pragma unroll
  for (int r = 0; r < 16; ++r) s[r] = __builtin_amdgcn_exp2f(s[r]);
  const bf16x8 p0 = pack8n(s, 0), p1 = pack8n(s, 1);
  o = MFMA32(v0, p0, o);
  o = MFMA32(v1, p1, o);
  l = MFMA16(ones, p0, l);
  l = MFMA16(ones, p1, l);
}

__global__ __launch_bounds__(768, 3) void k2_attn_conv(const unsigned short* aq, const unsigned short* ssk,
                                                       const unsigned short* ssd, char* ws, unsigned short* out) {
  extern __shared__ __attribute__((aligned(16))) char lds[];
  const unsigned lds0 = (unsigned)(uintptr_t)(LAS char*)lds;
  const int tid = threadIdx.x, lane = tid & 63;
  const int wid = __builtin_amdgcn_readfirstlane(tid >> 6);
  const int bx = blockIdx.x, xg = bx & 7, li = bx >> 3;
  const int head = xg * 4 + (li >> 3), qb = li & 7;
  const char* kvg = ws + WS_KV + (size_t)head * 262144;
  if (wid >= 8) {
    const int cw = wid - 8;
    const int chg = xg * 32 + li;
    const int ch = chg * 4 + cw;
    const unsigned cb = lds0 + K2_CV + cw * CV_SZ;
    const unsigned vl = lane * 16;
    auto issue = [&](int s, int buf) {
#pragma unroll
      for (int i = 0; i < 4; ++i)
        glds16(kvg + (size_t)s * K2_STAGE + (cw * 4 + i) * 1024, vl, lds0 + buf * K2_STAGE + (cw * 4 + i) * 1024);
    };
    issue(0, 0); issue(1, 1);
    {
      const char* urg = ws + WS_UR + (size_t)ch * 4096;
      const char* hg = (const char*)(ssk + (size_t)ch * 2048);
#pragma unroll
      for (int i = 0; i < 4; ++i) glds16(urg + i * 1024, vl, cb + CV_URE + i * 1024);
#pragma unroll
      for (int i = 0; i < 4; ++i) glds16(hg + i * 1024, vl, cb + CV_HP + 480 + i * 1024);
      if (lane < 30) *(LAS u32x4*)(uintptr_t)(cb + CV_HP + lane * 16) = (u32x4){0u, 0u, 0u, 0u};
      if (lane < 4) *(LAS u32x4*)(uintptr_t)(cb + CV_URE + 4096 + lane * 16) = (u32x4){0u, 0u, 0u, 0u};
      if (lane < 4) *(LAS u32x4*)(uintptr_t)(cb + CV_URO + 4096 + lane * 16) = (u32x4){0u, 0u, 0u, 0u};
    }
    WAITV(12); BARRIER();
    f32x4 y[8];
#pragma unroll
    for (int n = 0; n < 8; ++n) y[n] = (f32x4){};
    const int i16 = lane & 15, g = lane >> 4;
    const unsigned ua = cb + ((i16 & 1) ? CV_URE : CV_URO) + (903 - (i16 >> 1) + 4 * g) * 4;
    const unsigned ha = cb + CV_HP + i16 * 32 + g * 16;
    int buf = 2;
    for (int s = 0; s < 16; ++s) {
      if (s + 2 < 16) { issue(s + 2, buf); buf = (buf == 2) ? 0 : buf + 1; }
      if (s == 0) {
        WAITV(4);
#pragma unroll
        for (int i = 0; i < 4; ++i) {
          const unsigned a = cb + CV_URE + i * 1024 + lane * 16;
          const u32x4 d = *(const LAS u32x4*)(uintptr_t)a;
          const unsigned nx = *(const LAS unsigned*)(uintptr_t)(a + 16);
          u32x4 o;
          o[0] = __builtin_amdgcn_alignbit(d[1], d[0], 16);
          o[1] = __builtin_amdgcn_alignbit(d[2], d[1], 16);
          o[2] = __builtin_amdgcn_alignbit(d[3], d[2], 16);
          o[3] = __builtin_amdgcn_alignbit(nx, d[3], 16);
          *(LAS u32x4*)(uintptr_t)(cb + CV_URO + i * 1024 + lane * 16) = o;
        }
      } else if (s <= 8) {
        const int rho = s - 1;
        bf16x8 uf[8], hf[8];
#pragma unroll
        for (int d = 0; d < 8; ++d) {
          const unsigned a = ua - (unsigned)(8 * d - rho) * 64;
          u32x4 t;
#pragma unroll
          for (int j = 0; j < 4; ++j) t[j] = *(const LAS unsigned*)(uintptr_t)(a + j * 4);
          uf[d] = __builtin_bit_cast(bf16x8, t);
          hf[d] = lds_rd128(ha + (8 * d + rho) * 64);
        }
#pragma unroll
        for (int n = 0; n < 8; ++n)
#pragma unroll
          for (int p = 0; p <= n; ++p) y[n] = MFMA16(uf[n - p], hf[p], y[n]);
      } else if (s == 9) {
#pragma unroll
        for (int n = 0; n < 8; ++n) {
          u32x2 o = {pk2(y[n][0], y[n][1]), pk2(y[n][2], y[n][3])};
          *(LAS u32x2*)(uintptr_t)(cb + CV_Z + (256 * n + 16 * i16 + 4 * g) * 2) = o;
        }
      } else if (s <= 11) {
        const int t0 = ((s - 10) * 256 + cw * 64 + lane) * 4;
        const u32x2 dd = *(const u32x2*)(ssd + chg * 4);
        const float dv[4] = {bflo(dd[0]), bfhi(dd[0]), bflo(dd[1]), bfhi(dd[1])};
        u32x2 qv[4];
#pragma unroll
        for (int r = 0; r < 4; ++r) qv[r] = *(const u32x2*)(aq + (size_t)(t0 + r) * 1024 + chg * 4);
        float z[4][4];
#pragma unroll
        for (int c = 0; c < 4; ++c) {
          const unsigned cbc = lds0 + K2_CV + c * CV_SZ;
          const u32x2 yy = *(const LAS u32x2*)(uintptr_t)(cbc + CV_Z + t0 * 2);
          const u32x2 uu = *(const LAS u32x2*)(uintptr_t)(cbc + CV_URE + (2044 - t0) * 2);
          const float yv[4] = {bflo(yy[0]), bfhi(yy[0]), bflo(yy[1]), bfhi(yy[1])};
          const float uv[4] = {bfhi(uu[1]), bflo(uu[1]), bfhi(uu[0]), bflo(uu[0])};
#pragma unroll
          for (int r = 0; r < 4; ++r) z[r][c] = rbf(yv[r] + rbf(uv[r] * dv[c]));
        }
#pragma unroll
        for (int r = 0; r < 4; ++r) {
          u32x2 o = {pk2(bflo(qv[r][0]) * z[r][0], bfhi(qv[r][0]) * z[r][1]),
                     pk2(bflo(qv[r][1]) * z[r][2], bfhi(qv[r][1]) * z[r][3])};
          *(u32x2*)(out + (size_t)(t0 + r) * 2048 + chg * 4) = o;
        }
      }
      if (s + 2 < 16) WAITV(4) ; else WAITV(0);
      if (s >= 10 && s <= 11) WAITV(0);
      BARRIER_L();
    }
    return;
  }
  const int tb = qb * 8 + wid;
  const char* qg = ws + WS_Q + ((size_t)(head * 64 + tb) * 2) * 1024 + lane * 16;
  const bf16x8 q0 = *(const bf16x8*)qg, q1 = *(const bf16x8*)(qg + 1024);
  float km = ((const float*)(ws + WS_KMAX))[head * 32 + (lane & 31)];
#pragma unroll
  for (int o = 1; o < 32; o <<= 1) km = fmaxf(km, __shfl_xor(km, o));
  float qs = 0.f;
  {
    const u32x4 a = __builtin_bit_cast(u32x4, q0), b = __builtin_bit_cast(u32x4, q1);
#pragma unroll
    for (int e = 0; e < 4; ++e) {
      qs += bflo(a[e]) * bflo(a[e]) + bfhi(a[e]) * bfhi(a[e]);
      qs += bflo(b[e]) * bflo(b[e]) + bfhi(b[e]) * bfhi(b[e]);
    }
    qs = xhalf_sum(qs);
  }
  const float mb = sqrtf(qs * km) * 1.0009765625f;
  f32x16 negm;
#pragma unroll
  for (int r = 0; r < 16; ++r) negm[r] = -mb;
  bf16x8 ones;
  {
    const short one = (((lane & 15) == ((lane >> 4) & 1))) ? (short)0x3f80 : (short)0;
#pragma unroll
    for (int e = 0; e < 8; ++e) ones[e] = one;
  }
  f32x16 o = (f32x16){};
  f32x4 l = (f32x4){};
  BARRIER();
  unsigned so = lds0 + lane * 16;
  for (int s = 0; s < 16; ++s) {
#pragma unroll
    for (int t = 0; t < 4; ++t) {
      const unsigned a = so + t * 4096;
      attn_tile(lds_rd128(a), lds_rd128(a + 1024), lds_rd128(a + 2048), lds_rd128(a + 3072), q0, q1, negm, ones, o, l);
    }
    so = (so >= lds0 + 2 * K2_STAGE) ? so - 2 * K2_STAGE : so + K2_STAGE;
    BARRIER_L();
  }
  float lq;
  {
    const float a = __shfl(l[0], lane & 15), b = __shfl(l[1], lane & 15);
    lq = (lane & 16) ? b : a;
  }
  if (__builtin_expect(!__all(lq >= 9.094947017729282e-13f && lq < 3.0e38f), 0)) {
    float mx = -3.0e38f;
    const f32x16 zero = (f32x16){};
    const char* kp = kvg + lane * 16;
    for (int kb = 0; kb < 64; ++kb) {
      f32x16 s = MFMA32(*(const bf16x8*)(kp + kb * 4096), q0, zero);
      s = MFMA32(*(const bf16x8*)(kp + kb * 4096 + 1024), q1, s);
#pragma unroll
      for (int r = 0; r < 16; ++r) mx = fmaxf(mx, s[r]);
    }
    mx = xhalf_max(mx);
#pragma unroll
    for (int r = 0; r < 16; ++r) negm[r] = -mx;
    o = (f32x16){};
    l = (f32x4){};
    for (int kb = 0; kb < 64; ++kb) {
      const char* a = kp + kb * 4096;
      attn_tile(*(const bf16x8*)a, *(const bf16x8*)(a + 1024), *(const bf16x8*)(a + 2048), *(const bf16x8*)(a + 3072),
                q0, q1, negm, ones, o, l);
    }
    const float a = __shfl(l[0], lane & 15), b = __shfl(l[1], lane & 15);
    lq = (lane & 16) ? b : a;
  }
  const float inv = 1.0f / lq;
  char* od = ws + WS_O + ((size_t)(tb * 64 + head * 2)) * 1024 + lane * 16;
  *(bf16x8*)od = pack8(o, 0, inv);
  *(bf16x8*)(od + 1024) = pack8(o, 1, inv);
}

constexpr int K3_LDS = 131072;
__global__ __launch_bounds__(512, 2) void k3_oproj(const unsigned short* wo, const char* ws, unsigned short* out) {
  extern __shared__ __attribute__((aligned(16))) char lds[];
  const unsigned lds0 = (unsigned)(uintptr_t)(LAS char*)lds;
  const int tid = threadIdx.x, lane = tid & 63;
  const int wid = __builtin_amdgcn_readfirstlane(tid >> 6);
  const int bx = blockIdx.x, xg = bx & 7, li = bx >> 3;
  const int mt = 4 * (xg & 3) + (li & 3), nt = 8 * (xg >> 2) + (li >> 2);
  if (wid >= 4) {
    const int lw = wid - 4;
    const unsigned vw = ((lane >> 3) * 1024 + (((lane & 7) ^ (((lane >> 4) & 1) << 2)) * 8)) * 2;
    const char* wb = (const char*)(wo + nt * 64);
#pragma unroll
    for (int c = 0; c < 8; ++c)
#pragma unroll
      for (int i = 0; i < 4; ++i) {
        const int p = c * 16 + lw * 4 + i;
        glds16(wb + (size_t)p * 8 * 2048, vw, lds0 + p * 1024);
      }
    WAITV(28); BARRIER(); WAITV(24); BARRIER(); WAITV(20); BARRIER(); WAITV(16); BARRIER();
    WAITV(12); BARRIER(); WAITV(8); BARRIER(); WAITV(4); BARRIER(); WAITV(0); BARRIER();
    return;
  }
  const int tb = mt * 4 + wid;
  const char* og = ws + WS_O + (size_t)tb * 65536 + lane * 16;
  const int hi = lane >> 5, q4 = (lane & 15) >> 2;
  const unsigned wa0 = lds0 + (hi * 4 + q4) * 128 + ((0 ^ (q4 >> 1)) * 64) + ((lane >> 4) & 1) * 32 + (lane & 3) * 8;
  const unsigned wa1 = lds0 + (hi * 4 + q4) * 128 + ((1 ^ (q4 >> 1)) * 64) + ((lane >> 4) & 1) * 32 + (lane & 3) * 8;
  f32x16 acc0 = (f32x16){}, acc1 = (f32x16){};
  bf16x8 of[2][8];
#pragma unroll
  for (int j = 0; j < 8; ++j) of[0][j] = *(const bf16x8*)(og + j * 1024);
#pragma unroll
  for (int c = 0; c < 8; ++c) {
    if (c < 7) {
#pragma unroll
      for (int j = 0; j < 8; ++j) of[(c + 1) & 1][j] = *(const bf16x8*)(og + ((c + 1) * 8 + j) * 1024);
    }
    BARRIER();
#pragma unroll
    for (int j = 0; j < 8; ++j) {
      const unsigned ko = (c * 8 + j) * 2048;
      const bf16x8 w0 = cat44(lds_tr(wa0 + ko), lds_tr(wa0 + ko + 1024));
      const bf16x8 w1 = cat44(lds_tr(wa1 + ko), lds_tr(wa1 + ko + 1024));
      acc0 = MFMA32(w0, of[c & 1][j], acc0);
      acc1 = MFMA32(w1, of[c & 1][j], acc1);
    }
  }
  unsigned short* od = out + (size_t)(tb * 32 + (lane & 31)) * 2048 + 1024 + nt * 64 + hi * 4;
#pragma unroll
  for (int gq = 0; gq < 4; ++gq) {
    u32x2 a = {pk2(acc0[4 * gq], acc0[4 * gq + 1]), pk2(acc0[4 * gq + 2], acc0[4 * gq + 3])};
    u32x2 b = {pk2(acc1[4 * gq], acc1[4 * gq + 1]), pk2(acc1[4 * gq + 2], acc1[4 * gq + 3])};
    *(u32x2*)(od + gq * 8) = a;
    *(u32x2*)(od + 32 + gq * 8) = b;
  }
}

extern "C" void kernel_launch(void* const* d_in, const int* in_sizes, int n_in, void* d_out, int out_size,
                              void* d_ws, size_t ws_size, hipStream_t stream) {
  const unsigned short* aq = (const unsigned short*)d_in[0];
  const unsigned short* ak = (const unsigned short*)d_in[1];
  const unsigned short* av = (const unsigned short*)d_in[2];
  const unsigned short* shk = (const unsigned short*)d_in[3];
  const unsigned short* ssk = (const unsigned short*)d_in[4];
  const unsigned short* ssd = (const unsigned short*)d_in[5];
  const unsigned short* x = (const unsigned short*)d_in[6];
  const unsigned short* wqkv = (const unsigned short*)d_in[7];
  const unsigned short* wo = (const unsigned short*)d_in[8];
  char* ws = (char*)d_ws;
  unsigned short* out = (unsigned short*)d_out;
  (void)hipFuncSetAttribute((const void*)k1_qkv, hipFuncAttributeMaxDynamicSharedMemorySize, K1_LDS);
  (void)hipFuncSetAttribute((const void*)k2_attn_conv, hipFuncAttributeMaxDynamicSharedMemorySize, K2_LDS);
  (void)hipFuncSetAttribute((const void*)k3_oproj, hipFuncAttributeMaxDynamicSharedMemorySize, K3_LDS);
  k1_qkv<<<256, 512, K1_LDS, stream>>>(x, wqkv, ak, av, shk, ws);
  k2_attn_conv<<<256, 768, K2_LDS, stream>>>(aq, ssk, ssd, ws, out);
  k3_oproj<<<256, 512, K3_LDS, stream>>>(wo, ws, out);
}
```

```cpp
#include <hip/hip_runtime.h>
#include <hip/hip_bf16.h>
#include <stdint.h>

typedef short bf16x8 __attribute__((ext_vector_type(8)));
typedef short s16x4 __attribute__((ext_vector_type(4)));
typedef float f32x16 __attribute__((ext_vector_type(16)));
typedef float f32x4 __attribute__((ext_vector_type(4)));
typedef unsigned u32x4 __attribute__((ext_vector_type(4)));
typedef unsigned u32x2 __attribute__((ext_vector_type(2)));
typedef __bf16 bf16v2 __attribute__((ext_vector_type(2)));
typedef float f32x2 __attribute__((ext_vector_type(2)));
#define LAS __attribute__((address_space(3)))
#define DI __device__ __forceinline__
#define MFMA32(a, b, c) __builtin_amdgcn_mfma_f32_32x32x16_bf16((a), (b), (c), 0, 0, 0)
#define MFMA16(a, b, c) __builtin_amdgcn_mfma_f32_16x16x32_bf16((a), (b), (c), 0, 0, 0)

constexpr int S_ = 2048, D_ = 1024, NH = 32;
constexpr size_t WS_Q = 0;
constexpr size_t WS_KV = WS_Q + (4u << 20);
constexpr size_t WS_O = WS_KV + (8u << 20);
constexpr size_t WS_UR = WS_O + (4u << 20);
constexpr size_t WS_KMAX = WS_UR + (4u << 20);

DI unsigned pk2(float lo, float hi) {
  f32x2 v = {lo, hi};
  bf16v2 b = __builtin_convertvector(v, bf16v2);
  return __builtin_bit_cast(unsigned, b);
}
DI float bflo(unsigned u) { return __uint_as_float(u << 16); }
DI float bfhi(unsigned u) { return __uint_as_float(u & 0xffff0000u); }
DI float rbf(float x) { return bflo(pk2(x, x)); }
DI bf16x8 pack8(const f32x16& x, int s, float scale) {
  u32x4 p;
  p[0] = pk2(x[8 * s + 0] * scale, x[8 * s + 1] * scale);
  p[1] = pk2(x[8 * s + 2] * scale, x[8 * s + 3] * scale);
  p[2] = pk2(x[8 * s + 4] * scale, x[8 * s + 5] * scale);
  p[3] = pk2(x[8 * s + 6] * scale, x[8 * s + 7] * scale);
  return __builtin_bit_cast(bf16x8, p);
}
DI bf16x8 pack8n(const f32x16& x, int s) {
  u32x4 p;
  p[0] = pk2(x[8 * s + 0], x[8 * s + 1]);
  p[1] = pk2(x[8 * s + 2], x[8 * s + 3]);
  p[2] = pk2(x[8 * s + 4], x[8 * s + 5]);
  p[3] = pk2(x[8 * s + 6], x[8 * s + 7]);
  return __builtin_bit_cast(bf16x8, p);
}
DI void glds16(const void* gbase, unsigned voff, unsigned lds_dst) {
  unsigned keep;
  asm volatile("s_mov_b32 %0, m0\n\ts_mov_b32 m0, %3\n\ts_nop 0\n\tglobal_load_lds_dwordx4 %1, %2\n\ts_mov_b32 m0, %0"
               : "=&s"(keep) : "v"(voff), "s"(gbase), "s"(lds_dst) : "memory");
}
DI bf16x8 lds_rd128(unsigned a) { return *(const LAS bf16x8*)(uintptr_t)a; }
DI s16x4 lds_tr(unsigned a) {
  typedef short v4i16_t __attribute__((ext_vector_type(4)));
  return __builtin_amdgcn_ds_read_tr16_b64_v4i16((LAS v4i16_t*)(uintptr_t)a);
}
DI bf16x8 cat44(s16x4 lo, s16x4 hi) { return __builtin_shufflevector(lo, hi, 0, 1, 2, 3, 4, 5, 6, 7); }
#define SBAR() __builtin_amdgcn_sched_barrier(0)
#define SGB(m, n) __builtin_amdgcn_sched_group_barrier((m), (n), 0)
#define WAITV(n) asm volatile("s_waitcnt vmcnt(" #n ")" ::: "memory")
#define BARRIER_L() asm volatile("s_waitcnt lgkmcnt(0)\n\ts_barrier" ::: "memory")
#define BARRIER() asm volatile("s_barrier" ::: "memory")
#define STAMP(k, i) do { } while (0)
DI float xhalf_sum(float v) {
  auto r = __builtin_amdgcn_permlane32_swap(__float_as_uint(v), __float_as_uint(v), false, false);
  return __uint_as_float(r[0]) + __uint_as_float(r[1]);
}
DI float xhalf_max(float v) {
  auto r = __builtin_amdgcn_permlane32_swap(__float_as_uint(v), __float_as_uint(v), false, false);
  return fmaxf(__uint_as_float(r[0]), __uint_as_float(r[1]));
}

constexpr int K1_STAGE = 40960, K1_LDS = 3 * K1_STAGE;
constexpr float QSCALE = 0.17677669529663687f * 1.4426950408889634f;

struct Frags { bf16x8 xf[2]; bf16x8 wf[3]; };

__global__ __launch_bounds__(512, 2) void k1_qkv(const unsigned short* x, const unsigned short* w,
                                                 const unsigned short* ak, const unsigned short* av,
                                                 const unsigned short* shk, char* ws) {
  extern __shared__ __attribute__((aligned(16))) char lds[];
  const unsigned lds0 = (unsigned)(uintptr_t)(LAS char*)lds;
  const int tid = threadIdx.x, lane = tid & 63;
  const int wid = __builtin_amdgcn_readfirstlane(tid >> 6);
  const int bx = blockIdx.x, xg = bx & 7, li = bx >> 3;
  const int mt = 8 * (xg & 1) + (li & 7), nt = 4 * (xg >> 1) + (li >> 3);
  STAMP(0, 0);
  if (wid >= 4) {
    const int lw = wid - 4;
    const unsigned rowl = lane >> 3, sl = lane & 7;
    const unsigned vx0 = (rowl * 1024 + ((sl ^ ((lane >> 4) & 7)) * 8)) * 2;
    const unsigned vx1 = (rowl * 1024 + ((sl ^ ((4 + (lane >> 4)) & 7)) * 8)) * 2;
    const unsigned vw = (rowl * 3072 + ((sl ^ (((lane >> 4) & 1) << 2)) * 8)) * 2;
    const char* xb = (const char*)(x + (size_t)(mt * 128 + lw * 32) * 1024);
    const char* wb = (const char*)(w + nt * 64);
    auto issue = [&](int s, int buf) {
      const unsigned dst = lds0 + buf * K1_STAGE;
      const char* xs = xb + s * 128;
#pragma unroll
      for (int i = 0; i < 4; ++i) glds16(xs + i * 8 * 2048, (i & 1) ? vx1 : vx0, dst + (lw * 4 + i) * 1024);
      const char* wsrc = wb + (size_t)s * 64 * 6144;
#pragma unroll
      for (int i = 0; i < 6; ++i) {
        const int q = lw * 6 + i, sec = q >> 3, p = q & 7;
        glds16(wsrc + (size_t)p * 8 * 6144 + sec * 2048, vw, dst + 16384 + q * 1024);
      }
    };
    issue(0, 0); issue(1, 1); issue(2, 2);
    WAITV(20); BARRIER();
    int buf = 0;
    for (int s = 0; s < 13; ++s) {
      WAITV(10); BARRIER();
      issue(s + 3, buf);
      buf = (buf == 2) ? 0 : buf + 1;
    }
    WAITV(10); BARRIER();
    WAITV(0); BARRIER();
    BARRIER();
    STAMP(0, 1);
    {
      const int lt = tid - 256;
      const int cg = lt & 7, tg = lt >> 3;
      const int ch0 = (bx & 15) * 64 + cg * 8, t0 = (bx >> 4) * 128 + tg * 4;
      u32x4 kr[7], vr[4], wk[4];
#pragma unroll
      for (int r = 0; r < 7; ++r) {
        const int t = t0 - 3 + r;
        kr[r] = (u32x4){0u, 0u, 0u, 0u};
        if (t >= 0) kr[r] = *(const u32x4*)(ak + (size_t)t * 1024 + ch0);
      }
#pragma unroll
      for (int r = 0; r < 4; ++r) vr[r] = *(const u32x4*)(av + (size_t)(t0 + r) * 1024 + ch0);
#pragma unroll
      for (int r = 0; r < 4; ++r) wk[r] = *(const u32x4*)(shk + (size_t)ch0 * 4 + r * 8);
      unsigned short* ur = (unsigned short*)(ws + WS_UR);
#pragma unroll
      for (int c = 0; c < 8; ++c) {
        const unsigned wa = wk[c >> 1][(c & 1) * 2], wb2 = wk[c >> 1][(c & 1) * 2 + 1];
        const float w0 = bflo(wa), w1 = bfhi(wa), w2 = bflo(wb2), w3 = bfhi(wb2);
        float kf[7], u[4];
#pragma unroll
        for (int r = 0; r < 7; ++r) kf[r] = (c & 1) ? bfhi(kr[r][c >> 1]) : bflo(kr[r][c >> 1]);
#pragma unroll
        for (int r = 0; r < 4; ++r) {
          float a = rbf(kf[r] * w3);
          a = rbf(a + rbf(kf[r + 1] * w2));
          a = rbf(a + rbf(kf[r + 2] * w1));
          a = rbf(a + rbf(kf[r + 3] * w0));
          const float vv = (c & 1) ? bfhi(vr[r][c >> 1]) : bflo(vr[r][c >> 1]);
          u[r] = a * vv;
        }
        u32x2 o = {pk2(u[3], u[2]), pk2(u[1], u[0])};
        *(u32x2*)(ur + (size_t)(ch0 + c) * 2048 + (2044 - t0)) = o;
      }
    }
    STAMP(0, 2);
    return;
  }
  const int wm = wid & 1, wn = wid >> 1;
  const int r32 = lane & 31, hi = lane >> 5;
  unsigned xa[4];
#pragma unroll
  for (int kk = 0; kk < 4; ++kk) xa[kk] = lds0 + (wm * 64 + r32) * 128 + (((2 * kk + hi) ^ ((r32 >> 1) & 7)) * 16);
  const int q4 = (lane & 15) >> 2;
  const unsigned wa = lds0 + 16384 + (hi * 8 + q4) * 128 + ((wn ^ (q4 >> 1)) * 64) + ((lane >> 4) & 1) * 32 + (lane & 3) * 8;
  f32x16 acc[2][3];
#pragma unroll
  for (int i = 0; i < 2; ++i)
#pragma unroll
    for (int j = 0; j < 3; ++j) acc[i][j] = (f32x16){};
  auto ld = [&](Frags& f, unsigned so, int kk) {
    f.xf[0] = lds_rd128(xa[kk] + so);
    f.xf[1] = lds_rd128(xa[kk] + so + 4096);
#pragma unroll
    for (int j = 0; j < 3; ++j)
      f.wf[j] = cat44(lds_tr(wa + so + j * 8192 + kk * 2048), lds_tr(wa + so + j * 8192 + kk * 2048 + 512));
  };
  auto mma = [&](const Frags& f) {
#pragma unroll
    for (int i = 0; i < 2; ++i) {
      acc[i][0] = MFMA32(f.wf[0], f.xf[i], acc[i][0]);
      acc[i][1] = MFMA32(f.wf[1], f.xf[i], acc[i][1]);
      acc[i][2] = MFMA32(f.xf[i], f.wf[2], acc[i][2]);
    }
  };
  Frags f0, f1;
  BARRIER();
  STAMP(0, 1);
  unsigned so = 0;
  ld(f0, so, 0);
#define K1_ILV() do { SGB(0x8, 1); SGB(0x100, 2); SGB(0x8, 1); SGB(0x100, 2); SGB(0x8, 1); SGB(0x100, 2); \
    SGB(0x8, 1); SGB(0x100, 2); SGB(0x8, 2); SBAR(); } while (0)
  SBAR();
  for (int s = 0; s < 15; ++s) {
    ld(f1, so, 1); mma(f0); K1_ILV();
    ld(f0, so, 2); mma(f1); K1_ILV();
    ld(f1, so, 3); mma(f0); K1_ILV();
    BARRIER_L();
    so = (so == 2 * K1_STAGE) ? 0 : so + K1_STAGE;
    SBAR();
    ld(f0, so, 0); mma(f1); K1_ILV();
  }
  ld(f1, so, 1); mma(f0); K1_ILV();
  ld(f0, so, 2); mma(f1); K1_ILV();
  ld(f1, so, 3); mma(f0); K1_ILV();
  BARRIER();
  mma(f1);
  STAMP(0, 2);
  const int head = nt * 2 + wn;
  float kn = 0.f;
#pragma unroll
  for (int i = 0; i < 2; ++i) {
    const int tb = mt * 4 + wm * 2 + i;
    char* qd = ws + WS_Q + ((size_t)(head * 64 + tb) * 2) * 1024 + lane * 16;
    char* kvd = ws + WS_KV + ((size_t)(head * 64 + tb) * 4) * 1024 + lane * 16;
    float ss = 0.f;
#pragma unroll
    for (int s = 0; s < 2; ++s) {
      *(bf16x8*)(qd + s * 1024) = pack8(acc[i][0], s, QSCALE);
      const bf16x8 kp = pack8n(acc[i][1], s);
      *(bf16x8*)(kvd + s * 1024) = kp;
      const u32x4 ku = __builtin_bit_cast(u32x4, kp);
#pragma unroll
      for (int e = 0; e < 4; ++e) { const float a = bflo(ku[e]), b = bfhi(ku[e]); ss += a * a + b * b; }
      *(bf16x8*)(kvd + (2 + s) * 1024) = pack8n(acc[i][2], s);
    }
    kn = fmaxf(kn, xhalf_sum(ss));
  }
#pragma unroll
  for (int o = 1; o < 32; o <<= 1) kn = fmaxf(kn, __shfl_xor(kn, o));
  if (lane == 0) ((float*)(ws + WS_KMAX))[head * 32 + mt * 2 + wm] = kn;
  STAMP(0, 3);
}

constexpr int K2_STAGE = 16384, K2_NST = 4;
constexpr int CV_URE = 0, CV_URO = 4160, CV_HP = 8320, CV_Z = 12928, CV_SZ = 17024;
constexpr int K2_CV = K2_NST * K2_STAGE, K2_LDS = K2_CV + 4 * CV_SZ;

DI void attn_tile(const bf16x8 k0, const bf16x8 k1, const bf16x8 v0, const bf16x8 v1, const bf16x8 q0, const bf16x8 q1,
                  const f32x16& negm, const bf16x8 ones, f32x16& o, f32x4& l) {
  f32x16 s = MFMA32(k0, q0, negm);
  s = MFMA32(k1, q1, s);
#pragma unroll
  for (int r = 0; r < 16; ++r) s[r] = __builtin_amdgcn_exp2f(s[r]);
  const bf16x8 p0 = pack8n(s, 0), p1 = pack8n(s, 1);
  o = MFMA32(v0, p0, o);
  o = MFMA32(v1, p1, o);
  l = MFMA16(ones, p0, l);
  l = MFMA16(ones, p1, l);
}

__global__ __launch_bounds__(768, 3) void k2_attn_conv(const unsigned short* aq, const unsigned short* ssk,
                                                       const unsigned short* ssd, char* ws, unsigned short* out) {
  extern __shared__ __attribute__((aligned(16))) char lds[];
  const unsigned lds0 = (unsigned)(uintptr_t)(LAS char*)lds;
  const int tid = threadIdx.x, lane = tid & 63;
  const int wid = __builtin_amdgcn_readfirstlane(tid >> 6);
  const int bx = blockIdx.x, xg = bx & 7, li = bx >> 3;
  const int head = xg * 4 + (li >> 3), qb = li & 7;
  const char* kvg = ws + WS_KV + (size_t)head * 262144;
  const unsigned vl = lane * 16;
  STAMP(1, 0);
  if (wid >= 8) {
    const int cw = wid - 8;
    const int chg = xg * 32 + li;
    const int ch = chg * 4 + cw;
    const unsigned cb = lds0 + K2_CV + cw * CV_SZ;
    {
      const char* urg = ws + WS_UR + (size_t)ch * 4096;
      const char* hg = (const char*)(ssk + (size_t)ch * 2048);
#pragma unroll
      for (int i = 0; i < 4; ++i) glds16(urg + i * 1024, vl, cb + CV_URE + i * 1024);
#pragma unroll
      for (int i = 0; i < 4; ++i) glds16(hg + i * 1024, vl, cb + CV_HP + 480 + i * 1024);
      if (lane < 30) *(LAS u32x4*)(uintptr_t)(cb + CV_HP + lane * 16) = (u32x4){0u, 0u, 0u, 0u};
      if (lane < 4) *(LAS u32x4*)(uintptr_t)(cb + CV_URE + 4096 + lane * 16) = (u32x4){0u, 0u, 0u, 0u};
      if (lane < 4) *(LAS u32x4*)(uintptr_t)(cb + CV_URO + 4096 + lane * 16) = (u32x4){0u, 0u, 0u, 0u};
    }
    BARRIER();
    BARRIER();
    BARRIER();
    WAITV(0);
#pragma unroll
    for (int i = 0; i < 4; ++i) {
      const unsigned a = cb + CV_URE + i * 1024 + lane * 16;
      const u32x4 d = *(const LAS u32x4*)(uintptr_t)a;
      const unsigned nx = *(const LAS unsigned*)(uintptr_t)(a + 16);
      u32x4 o;
      o[0] = __builtin_amdgcn_alignbit(d[1], d[0], 16);
      o[1] = __builtin_amdgcn_alignbit(d[2], d[1], 16);
      o[2] = __builtin_amdgcn_alignbit(d[3], d[2], 16);
      o[3] = __builtin_amdgcn_alignbit(nx, d[3], 16);
      *(LAS u32x4*)(uintptr_t)(cb + CV_URO + i * 1024 + lane * 16) = o;
    }
    BARRIER();
    f32x4 y[8];
#pragma unroll
    for (int n = 0; n < 8; ++n) y[n] = (f32x4){};
    const int i16 = lane & 15, g = lane >> 4;
    const unsigned ua = cb + ((i16 & 1) ? CV_URE : CV_URO) + (903 - (i16 >> 1) + 4 * g) * 4;
    const unsigned ha = cb + CV_HP + i16 * 32 + g * 16;
    for (int rho = 0; rho < 8; ++rho) {
      bf16x8 uf[8], hf[8];
#pragma unroll
      for (int d = 0; d < 8; ++d) {
        const unsigned a = ua - (unsigned)(8 * d - rho) * 64;
        u32x4 t;
#pragma unroll
        for (int j = 0; j < 4; ++j) t[j] = *(const LAS unsigned*)(uintptr_t)(a + j * 4);
        uf[d] = __builtin_bit_cast(bf16x8, t);
        hf[d] = lds_rd128(ha + (8 * d + rho) * 64);
      }
#pragma unroll
      for (int n = 0; n < 8; ++n)
#pragma unroll
        for (int p = 0; p <= n; ++p) y[n] = MFMA16(uf[n - p], hf[p], y[n]);
      BARRIER();
    }
#pragma unroll
    for (int n = 0; n < 8; ++n) {
      u32x2 o = {pk2(y[n][0], y[n][1]), pk2(y[n][2], y[n][3])};
      *(LAS u32x2*)(uintptr_t)(cb + CV_Z + (256 * n + 16 * i16 + 4 * g) * 2) = o;
    }
    u32x2 qv[2][4];
#pragma unroll
    for (int h = 0; h < 2; ++h)
#pragma unroll
      for (int r = 0; r < 4; ++r)
        qv[h][r] = *(const u32x2*)(aq + (size_t)((h * 256 + cw * 64 + lane) * 4 + r) * 1024 + chg * 4);
    const u32x2 dd = *(const u32x2*)(ssd + chg * 4);
    BARRIER_L();
    BARRIER();
    BARRIER();
    STAMP(1, 1);
    const float dv[4] = {bflo(dd[0]), bfhi(dd[0]), bflo(dd[1]), bfhi(dd[1])};
#pragma unroll
    for (int h = 0; h < 2; ++h) {
      const int t0 = (h * 256 + cw * 64 + lane) * 4;
      float z[4][4];
#pragma unroll
      for (int c = 0; c < 4; ++c) {
        const unsigned cbc = lds0 + K2_CV + c * CV_SZ;
        const u32x2 yy = *(const LAS u32x2*)(uintptr_t)(cbc + CV_Z + t0 * 2);
        const u32x2 uu = *(const LAS u32x2*)(uintptr_t)(cbc + CV_URE + (2044 - t0) * 2);
        const float yv[4] = {bflo(yy[0]), bfhi(yy[0]), bflo(yy[1]), bfhi(yy[1])};
        const float uv[4] = {bfhi(uu[1]), bflo(uu[1]), bfhi(uu[0]), bflo(uu[0])};
#pragma unroll
        for (int r = 0; r < 4; ++r) z[r][c] = rbf(yv[r] + rbf(uv[r] * dv[c]));
      }
#pragma unroll
      for (int r = 0; r < 4; ++r) {
        u32x2 o = {pk2(bflo(qv[h][r][0]) * z[r][0], bfhi(qv[h][r][0]) * z[r][1]),
                   pk2(bflo(qv[h][r][1]) * z[r][2], bfhi(qv[h][r][1]) * z[r][3])};
        *(u32x2*)(out + (size_t)(t0 + r) * 2048 + chg * 4) = o;
      }
    }
    STAMP(1, 2);
    return;
  }
  auto issue = [&](int s) {
    const unsigned dst = lds0 + (s & 3) * K2_STAGE + wid * 2048;
    glds16(kvg + (size_t)s * K2_STAGE + wid * 2048, vl, dst);
    glds16(kvg + (size_t)s * K2_STAGE + wid * 2048 + 1024, vl, dst + 1024);
  };
  issue(0); issue(1);
  const int tb = qb * 8 + wid;
  const char* qg = ws + WS_Q + ((size_t)(head * 64 + tb) * 2) * 1024 + lane * 16;
  const bf16x8 q0 = *(const bf16x8*)qg, q1 = *(const bf16x8*)(qg + 1024);
  float km = ((const float*)(ws + WS_KMAX))[head * 32 + (lane & 31)];
#pragma unroll
  for (int o = 1; o < 32; o <<= 1) km = fmaxf(km, __shfl_xor(km, o));
  float qs = 0.f;
  {
    const u32x4 a = __builtin_bit_cast(u32x4, q0), b = __builtin_bit_cast(u32x4, q1);
#pragma unroll
    for (int e = 0; e < 4; ++e) {
      qs += bflo(a[e]) * bflo(a[e]) + bfhi(a[e]) * bfhi(a[e]);
      qs += bflo(b[e]) * bflo(b[e]) + bfhi(b[e]) * bfhi(b[e]);
    }
    qs = xhalf_sum(qs);
  }
  float mb = sqrtf(qs * km) * 1.0009765625f;
  asm volatile("" : "+v"(mb));
  issue(2); issue(3);
  f32x16 negm;
#pragma unroll
  for (int r = 0; r < 16; ++r) negm[r] = -mb;
  asm volatile("" : "+v"(negm));
  bf16x8 ones;
  {
    const short one = (((lane & 15) == ((lane >> 4) & 1))) ? (short)0x3f80 : (short)0;
#pragma unroll
    for (int e = 0; e < 8; ++e) ones[e] = one;
  }
  f32x16 o = (f32x16){};
  f32x4 l = (f32x4){};
  WAITV(4); BARRIER();
  STAMP(1, 1);
  const unsigned lb = lds0 + lane * 16;
  bf16x8 k0 = lds_rd128(lb), k1 = lds_rd128(lb + 1024), v0 = lds_rd128(lb + 2048), v1;
  f32x16 sA = MFMA32(k0, q0, negm), sB;
  sA = MFMA32(k1, q1, sA);
  k0 = lds_rd128(lb + 4096); k1 = lds_rd128(lb + 4096 + 1024);
  u32x4 p0, p1 = (u32x4){0u, 0u, 0u, 0u};
  v1 = ones;
#define EXP4(S, i) do { S[i] = __builtin_amdgcn_exp2f(S[i]); S[i + 1] = __builtin_amdgcn_exp2f(S[i + 1]); \
    S[i + 2] = __builtin_amdgcn_exp2f(S[i + 2]); S[i + 3] = __builtin_amdgcn_exp2f(S[i + 3]); } while (0)
#define EXP2(S, i) do { S[i] = __builtin_amdgcn_exp2f(S[i]); S[i + 1] = __builtin_amdgcn_exp2f(S[i + 1]); } while (0)
#define PK4(P, S, i) do { P[0] = pk2(S[i], S[i + 1]); P[1] = pk2(S[i + 2], S[i + 3]); P[2] = pk2(S[i + 4], S[i + 5]); \
    P[3] = pk2(S[i + 6], S[i + 7]); } while (0)
#define BF(P) __builtin_bit_cast(bf16x8, P)
#define PIN(x) asm volatile("" : "+v"(x))
#define TILE(SC, SN, a0, a1, a2) do { \
    SN = MFMA32(k0, q0, negm); k0 = lds_rd128(a2); EXP4(SC, 0); PIN(SC); SBAR(); \
    o = MFMA32(v1, BF(p1), o); v1 = lds_rd128((a0) + 3072); EXP4(SC, 4); PIN(SC); SBAR(); \
    SN = MFMA32(k1, q1, SN); k1 = lds_rd128((a2) + 1024); PK4(p0, SC, 0); EXP2(SC, 8); PIN(SC); PIN(p0); SBAR(); \
    l = MFMA16(ones, BF(p1), l); EXP2(SC, 10); PIN(SC); SBAR(); \
    o = MFMA32(v0, BF(p0), o); v0 = lds_rd128((a1) + 2048); EXP4(SC, 12); PIN(SC); SBAR(); \
    l = MFMA16(ones, BF(p0), l); PK4(p1, SC, 8); PIN(p1); SBAR(); \
  } while (0)
  SBAR();
  for (int s = 0; s < 16; ++s) {
    const unsigned a = lb + (s & 3) * K2_STAGE, an = lb + ((s + 1) & 3) * K2_STAGE;
    TILE(sA, sB, a, a + 4096, a + 8192);
    TILE(sB, sA, a + 4096, a + 8192, a + 12288);
    TILE(sA, sB, a + 8192, a + 12288, an);
    TILE(sB, sA, a + 12288, an, an + 4096);
    if (s < 13) { WAITV(2); BARRIER_L(); if (s < 12) issue(s + 4); }
    else if (s == 13) { WAITV(0); BARRIER_L(); }
    SBAR();
  }
  o = MFMA32(v1, BF(p1), o);
  l = MFMA16(ones, BF(p1), l);
  STAMP(1, 2);
  float lq;
  {
    const float a = __shfl(l[0], lane & 15), b = __shfl(l[1], lane & 15);
    lq = (lane & 16) ? b : a;
  }
  if (__builtin_expect(!__all(lq >= 9.094947017729282e-13f && lq < 3.0e38f), 0)) {
    float mx = -3.0e38f;
    const f32x16 zero = (f32x16){};
    const char* kp = kvg + lane * 16;
    for (int kb = 0; kb < 64; ++kb) {
      f32x16 s = MFMA32(*(const bf16x8*)(kp + kb * 4096), q0, zero);
      s = MFMA32(*(const bf16x8*)(kp + kb * 4096 + 1024), q1, s);
#pragma unroll
      for (int r = 0; r < 16; ++r) mx = fmaxf(mx, s[r]);
    }
    mx = xhalf_max(mx);
#pragma unroll
    for (int r = 0; r < 16; ++r) negm[r] = -mx;
    o = (f32x16){};
    l = (f32x4){};
    for (int kb = 0; kb < 64; ++kb) {
      const char* a = kp + kb * 4096;
      attn_tile(*(const bf16x8*)a, *(const bf16x8*)(a + 1024), *(const bf16x8*)(a + 2048), *(const bf16x8*)(a + 3072),
                q0, q1, negm, ones, o, l);
    }
    const float a = __shfl(l[0], lane & 15), b = __shfl(l[1], lane & 15);
    lq = (lane & 16) ? b : a;
  }
  const float inv = 1.0f / lq;
  char* od = ws + WS_O + ((size_t)(tb * 64 + head * 2)) * 1024 + lane * 16;
  *(bf16x8*)od = pack8(o, 0, inv);
  *(bf16x8*)(od + 1024) = pack8(o, 1, inv);
  STAMP(1, 3);
}

constexpr int K3_LDS = 131072;
__global__ __launch_bounds__(512, 2) void k3_oproj(const unsigned short* wo, char* ws, unsigned short* out) {
  extern __shared__ __attribute__((aligned(16))) char lds[];
  const unsigned lds0 = (unsigned)(uintptr_t)(LAS char*)lds;
  const int tid = threadIdx.x, lane = tid & 63;
  const int wid = __builtin_amdgcn_readfirstlane(tid >> 6);
  const int bx = blockIdx.x, xg = bx & 7, li = bx >> 3;
  const int mt = 4 * (xg & 3) + (li & 3), nt = 8 * (xg >> 2) + (li >> 2);
  STAMP(2, 0);
  if (wid >= 4) {
    const int lw = wid - 4;
    const unsigned vw = ((lane >> 3) * 1024 + (((lane & 7) ^ (((lane >> 4) & 1) << 2)) * 8)) * 2;
    const char* wb = (const char*)(wo + nt * 64);
#pragma unroll
    for (int c = 0; c < 8; ++c)
#pragma unroll
      for (int i = 0; i < 4; ++i) {
        const int p = c * 16 + lw * 4 + i;
        glds16(wb + (size_t)p * 8 * 2048, vw, lds0 + p * 1024);
      }
    WAITV(28); BARRIER(); WAITV(24); BARRIER(); WAITV(20); BARRIER(); WAITV(16); BARRIER();
    WAITV(12); BARRIER(); WAITV(8); BARRIER(); WAITV(4); BARRIER(); WAITV(0); BARRIER();
    STAMP(2, 1);
    return;
  }
  const int tb = mt * 4 + wid;
  const char* og = ws + WS_O + (size_t)tb * 65536 + lane * 16;
  const int hi = lane >> 5, q4 = (lane & 15) >> 2;
  const unsigned wa0 = lds0 + (hi * 4 + q4) * 128 + ((0 ^ (q4 >> 1)) * 64) + ((lane >> 4) & 1) * 32 + (lane & 3) * 8;
  const unsigned wa1 = lds0 + (hi * 4 + q4) * 128 + ((1 ^ (q4 >> 1)) * 64) + ((lane >> 4) & 1) * 32 + (lane & 3) * 8;
  f32x16 acc0 = (f32x16){}, acc1 = (f32x16){};
  constexpr int OD = 24;
  bf16x8 of[OD];
#pragma unroll
  for (int j = 0; j < OD; ++j) of[j] = *(const bf16x8*)(og + j * 1024);
  bf16x8 w0[4], w1[4];
  auto ldw = [&](int j) {
    const unsigned ko = j * 2048;
    w0[j & 3] = cat44(lds_tr(wa0 + ko), lds_tr(wa0 + ko + 1024));
    w1[j & 3] = cat44(lds_tr(wa1 + ko), lds_tr(wa1 + ko + 1024));
  };
  BARRIER();
  STAMP(2, 1);
  ldw(0); ldw(1); ldw(2);
  SBAR();
#pragma unroll
  for (int j = 0; j < 64; ++j) {
    if (((j + 3) & 7) == 0 && j + 3 < 64) BARRIER();
    if (j + 3 < 64) ldw(j + 3);
    acc0 = MFMA32(w0[j & 3], of[j % OD], acc0);
    acc1 = MFMA32(w1[j & 3], of[j % OD], acc1);
    if (j + OD < 64) of[j % OD] = *(const bf16x8*)(og + (j + OD) * 1024);
    SBAR();
  }
  STAMP(2, 2);
  unsigned short* od = out + (size_t)(tb * 32 + (lane & 31)) * 2048 + 1024 + nt * 64 + hi * 4;
#pragma unroll
  for (int gq = 0; gq < 4; ++gq) {
    u32x2 a = {pk2(acc0[4 * gq], acc0[4 * gq + 1]), pk2(acc0[4 * gq + 2], acc0[4 * gq + 3])};
    u32x2 b = {pk2(acc1[4 * gq], acc1[4 * gq + 1]), pk2(acc1[4 * gq + 2], acc1[4 * gq + 3])};
    *(u32x2*)(od + gq * 8) = a;
    *(u32x2*)(od + 32 + gq * 8) = b;
  }
  STAMP(2, 3);
}

extern "C" void kernel_launch(void* const* d_in, const int* in_sizes, int n_in, void* d_out, int out_size,
                              void* d_ws, size_t ws_size, hipStream_t stream) {
  const unsigned short* aq = (const unsigned short*)d_in[0];
  const unsigned short* ak = (const unsigned short*)d_in[1];
  const unsigned short* av = (const unsigned short*)d_in[2];
  const unsigned short* shk = (const unsigned short*)d_in[3];
  const unsigned short* ssk = (const unsigned short*)d_in[4];
  const unsigned short* ssd = (const unsigned short*)d_in[5];
  const unsigned short* x = (const unsigned short*)d_in[6];
  const unsigned short* wqkv = (const unsigned short*)d_in[7];
  const unsigned short* wo = (const unsigned short*)d_in[8];
  char* ws = (char*)d_ws;
  unsigned short* out = (unsigned short*)d_out;
  (void)hipFuncSetAttribute((const void*)k1_qkv, hipFuncAttributeMaxDynamicSharedMemorySize, K1_LDS);
  (void)hipFuncSetAttribute((const void*)k2_attn_conv, hipFuncAttributeMaxDynamicSharedMemorySize, K2_LDS);
  (void)hipFuncSetAttribute((const void*)k3_oproj, hipFuncAttributeMaxDynamicSharedMemorySize, K3_LDS);
  k1_qkv<<<256, 512, K1_LDS, stream>>>(x, wqkv, ak, av, shk, ws);
  k2_attn_conv<<<256, 768, K2_LDS, stream>>>(aq, ssk, ssd, ws, out);
  k3_oproj<<<256, 512, K3_LDS, stream>>>(wo, ws, out);
}
```

```cpp
#include <hip/hip_runtime.h>
#include <hip/hip_bf16.h>
#include <stdint.h>

typedef short bf16x8 __attribute__((ext_vector_type(8)));
typedef short s16x4 __attribute__((ext_vector_type(4)));
typedef float f32x16 __attribute__((ext_vector_type(16)));
typedef float f32x4 __attribute__((ext_vector_type(4)));
typedef unsigned u32x4 __attribute__((ext_vector_type(4)));
typedef unsigned u32x2 __attribute__((ext_vector_type(2)));
typedef __bf16 bf16v2 __attribute__((ext_vector_type(2)));
typedef float f32x2 __attribute__((ext_vector_type(2)));
#define LAS __attribute__((address_space(3)))
#define DI __device__ __forceinline__
#define MFMA32(a, b, c) __builtin_amdgcn_mfma_f32_32x32x16_bf16((a), (b), (c), 0, 0, 0)
#define MFMA16(a, b, c) __builtin_amdgcn_mfma_f32_16x16x32_bf16((a), (b), (c), 0, 0, 0)

constexpr int S_ = 2048, D_ = 1024, NH = 32;
constexpr size_t WS_Q = 0;
constexpr size_t WS_KV = WS_Q + (4u << 20);
constexpr size_t WS_O = WS_KV + (8u << 20);
constexpr size_t WS_UR = WS_O + (4u << 20);
constexpr size_t WS_KMAX = WS_UR + (4u << 20);

DI unsigned pk2(float lo, float hi) {
  f32x2 v = {lo, hi};
  bf16v2 b = __builtin_convertvector(v, bf16v2);
  return __builtin_bit_cast(unsigned, b);
}
DI float bflo(unsigned u) { return __uint_as_float(u << 16); }
DI float bfhi(unsigned u) { return __uint_as_float(u & 0xffff0000u); }
DI float rbf(float x) { return bflo(pk2(x, x)); }
DI bf16x8 pack8(const f32x16& x, int s, float scale) {
  u32x4 p;
  p[0] = pk2(x[8 * s + 0] * scale, x[8 * s + 1] * scale);
  p[1] = pk2(x[8 * s + 2] * scale, x[8 * s + 3] * scale);
  p[2] = pk2(x[8 * s + 4] * scale, x[8 * s + 5] * scale);
  p[3] = pk2(x[8 * s + 6] * scale, x[8 * s + 7] * scale);
  return __builtin_bit_cast(bf16x8, p);
}
DI bf16x8 pack8n(const f32x16& x, int s) {
  u32x4 p;
  p[0] = pk2(x[8 * s + 0], x[8 * s + 1]);
  p[1] = pk2(x[8 * s + 2], x[8 * s + 3]);
  p[2] = pk2(x[8 * s + 4], x[8 * s + 5]);
  p[3] = pk2(x[8 * s + 6], x[8 * s + 7]);
  return __builtin_bit_cast(bf16x8, p);
}
DI void glds16(const void* gbase, unsigned voff, unsigned lds_dst) {
  unsigned keep;
  asm volatile("s_mov_b32 %0, m0\n\ts_mov_b32 m0, %3\n\ts_nop 0\n\tglobal_load_lds_dwordx4 %1, %2\n\ts_mov_b32 m0, %0"
               : "=&s"(keep) : "v"(voff), "s"(gbase), "s"(lds_dst) : "memory");
}
DI bf16x8 lds_rd128(unsigned a) { return *(const LAS bf16x8*)(uintptr_t)a; }
DI s16x4 lds_tr(unsigned a) {
  typedef short v4i16_t __attribute__((ext_vector_type(4)));
  return __builtin_amdgcn_ds_read_tr16_b64_v4i16((LAS v4i16_t*)(uintptr_t)a);
}
DI bf16x8 cat44(s16x4 lo, s16x4 hi) { return __builtin_shufflevector(lo, hi, 0, 1, 2, 3, 4, 5, 6, 7); }
#define SBAR() __builtin_amdgcn_sched_barrier(0)
#define SGB(m, n) __builtin_amdgcn_sched_group_barrier((m), (n), 0)
#define WAITV(n) asm volatile("s_waitcnt vmcnt(" #n ")" ::: "memory")
#define BARRIER_L() asm volatile("s_waitcnt lgkmcnt(0)\n\ts_barrier" ::: "memory")
#define BARRIER() asm volatile("s_barrier" ::: "memory")
#define STAMP(k, i) do { } while (0)
#define STAMPS(k, i) do { } while (0)
DI float xhalf_sum(float v) {
  auto r = __builtin_amdgcn_permlane32_swap(__float_as_uint(v), __float_as_uint(v), false, false);
  return __uint_as_float(r[0]) + __uint_as_float(r[1]);
}
DI float xhalf_max(float v) {
  auto r = __builtin_amdgcn_permlane32_swap(__float_as_uint(v), __float_as_uint(v), false, false);
  return fmaxf(__uint_as_float(r[0]), __uint_as_float(r[1]));
}

constexpr int K1_STAGE = 40960, K1_LDS = 4 * K1_STAGE;
constexpr float QSCALE = 0.17677669529663687f * 1.4426950408889634f;
#define K1_MAP 0
#define K1_STAG 2
#define K1_PF 1
#define K1_PRIO 0
#define K1_ST 0
DI void st16(void* p, u32x4 v) {
  *(u32x4*)p = v;
}

__global__ __launch_bounds__(768, 3) void k1_qkv(const unsigned short* x, const unsigned short* w,
                                                 const unsigned short* ak, const unsigned short* av,
                                                 const unsigned short* shk, char* ws) {
  extern __shared__ __attribute__((aligned(16))) char lds[];
  const unsigned lds0 = (unsigned)(uintptr_t)(LAS char*)lds;
  const int tid = threadIdx.x, lane = tid & 63;
  const int wid = __builtin_amdgcn_readfirstlane(tid >> 6);
  const int bx = blockIdx.x, xg = bx & 7, li = bx >> 3;
  const int mt = 8 * (xg & 1) + (li & 7), nt = 4 * (xg >> 1) + (li >> 3);
  STAMP(0, 0);
#define K1_S0 (4 * (mt & 3))
  const int s0 = K1_S0;
  if (wid >= 8) {
    const int lt = tid - 512;
    const int cg = lt & 7, tg = lt >> 3;
    const int ch0 = (bx & 15) * 64 + cg * 8, t0 = (bx >> 4) * 128 + tg * 4;
    u32x4 kr[7], vr[4], wk[4];
    unsigned short* ur = (unsigned short*)(ws + WS_UR);
    auto ldk = [&](int r) { const int t = t0 - 3 + r; kr[r] = *(const u32x4*)(ak + (size_t)(t < 0 ? 0 : t) * 1024 + ch0); };
    auto ldv = [&](int r) { vr[r] = *(const u32x4*)(av + (size_t)(t0 + r) * 1024 + ch0); };
    auto ldw = [&](int r) { wk[r] = *(const u32x4*)(shk + (size_t)ch0 * 4 + r * 8); };
    SBAR(); BARRIER(); BARRIER(); SBAR();
    ldk(0); ldk(1); ldk(2); ldk(3);
    SBAR(); BARRIER(); SBAR();
    ldk(4); ldk(5); ldk(6); ldw(0);
    SBAR(); BARRIER(); SBAR();
    ldv(0); ldv(1); ldv(2); ldw(1);
    SBAR(); BARRIER(); SBAR();
    ldv(3); ldw(2); ldw(3);
    SBAR();
    BARRIER(); BARRIER(); BARRIER();
    SBAR();
    if (t0 == 0) { kr[0] = (u32x4){0u, 0u, 0u, 0u}; kr[1] = kr[0]; kr[2] = kr[0]; }
#pragma unroll
    for (int c = 0; c < 8; ++c) {
      const unsigned wa = wk[c >> 1][(c & 1) * 2], wb2 = wk[c >> 1][(c & 1) * 2 + 1];
      const float w0 = bflo(wa), w1 = bfhi(wa), w2 = bflo(wb2), w3 = bfhi(wb2);
      float kf[7], u[4];
#pragma unroll
      for (int r = 0; r < 7; ++r) kf[r] = (c & 1) ? bfhi(kr[r][c >> 1]) : bflo(kr[r][c >> 1]);
#pragma unroll
      for (int r = 0; r < 4; ++r) {
        const float a = fmaf(kf[r + 3], w0, fmaf(kf[r + 2], w1, fmaf(kf[r + 1], w2, kf[r] * w3)));
        const float vv = (c & 1) ? bfhi(vr[r][c >> 1]) : bflo(vr[r][c >> 1]);
        u[r] = a * vv;
      }
      u32x2 o = {pk2(u[3], u[2]), pk2(u[1], u[0])};
      *(u32x2*)(ur + (size_t)(ch0 + c) * 2048 + (2044 - t0)) = o;
      SBAR(); BARRIER(); SBAR();
    }
    STAMP(0, 2);
    return;
  }
  if (wid >= 4) {
    const int lw = wid - 4;
    const unsigned rowl = lane >> 3, sl = lane & 7;
    unsigned vx[4], vw[6];
#pragma unroll
    for (int i = 0; i < 4; ++i)
      vx[i] = (i * 8 + rowl) * 2048 + ((sl ^ (((i & 1) * 4 + (lane >> 4)) & 7)) * 16) - i * 1024;
#pragma unroll
    for (int i = 0; i < 6; ++i) {
      const int q = lw * 6 + i, sec = q >> 3, p = q & 7;
      vw[i] = (p * 8 + rowl) * 6144 + sec * 2048 + ((sl ^ (((lane >> 4) & 1) << 1) ^ ((i & 1) << 2)) * 16) - (i % 3) * 1024;
    }
    const char* xb = (const char*)(x + (size_t)(mt * 128 + lw * 32) * 1024);
    const char* wb = (const char*)(w + nt * 64);
    auto issue = [&](int s, int buf) {
      const int ks = (s + s0) & 15;
      const unsigned dst = lds0 + buf * K1_STAGE;
      const char* xs = xb + ks * 128;
      const char* wsrc = wb + (size_t)ks * 64 * 6144;
      asm volatile("s_mov_b32 m0, %0\n\ts_nop 0\n\t"
                   "global_load_lds_dwordx4 %3, %1\n\tglobal_load_lds_dwordx4 %4, %1 offset:1024\n\t"
                   "global_load_lds_dwordx4 %5, %1 offset:2048\n\tglobal_load_lds_dwordx4 %6, %1 offset:3072\n\t"
                   "s_mov_b32 m0, %13\n\ts_nop 0\n\t"
                   "global_load_lds_dwordx4 %7, %2\n\tglobal_load_lds_dwordx4 %8, %2 offset:1024\n\t"
                   "global_load_lds_dwordx4 %9, %2 offset:2048\n\t"
                   "s_mov_b32 m0, %14\n\ts_nop 0\n\t"
                   "global_load_lds_dwordx4 %10, %2\n\tglobal_load_lds_dwordx4 %11, %2 offset:1024\n\t"
                   "global_load_lds_dwordx4 %12, %2 offset:2048"
                   :: "s"(dst + lw * 4096), "s"(xs), "s"(wsrc), "v"(vx[0]), "v"(vx[1]), "v"(vx[2]), "v"(vx[3]),
                      "v"(vw[0]), "v"(vw[1]), "v"(vw[2]), "v"(vw[3]), "v"(vw[4]), "v"(vw[5]),
                      "s"(dst + 16384 + lw * 6144), "s"(dst + 16384 + lw * 6144 + 3072) : "memory");
    };
    issue(0, 0); issue(1, 1); issue(2, 2); issue(3, 3);
    WAITV(30); BARRIER();
    for (int s = 0; s < 12; ++s) {
      WAITV(20); BARRIER();
      issue(s + 4, s & 3);
    }
    WAITV(20); BARRIER();
    WAITV(10); BARRIER();
    WAITV(0); BARRIER();
    STAMP(0, 1);
    return;
  }
  const int wm = wid & 1, wn = wid >> 1;
  const int c16 = lane & 15, g = lane >> 4, q4 = c16 >> 2;
  const unsigned xa = lds0 + (wm * 64 + c16) * 128 + ((g ^ (c16 >> 1)) * 16);
  const unsigned wa = lds0 + 16384 + (8 * g + q4) * 128 + (((2 * wn) ^ ((q4 >> 1) | ((g & 1) << 1))) * 32) + (lane & 3) * 8;
  f32x4 acc[4][6];
#pragma unroll
  for (int i = 0; i < 4; ++i)
#pragma unroll
    for (int j = 0; j < 6; ++j) acc[i][j] = (f32x4){};
  bf16x8 xf[2][4], wf[6];
  unsigned xa0 = xa, xa1 = xa ^ 64, wa0 = wa, wa1 = wa ^ 32;
  auto ldx = [&](int kk, int i) { xf[kk][i] = lds_rd128((kk ? xa1 : xa0) + i * 2048); };
  auto ldw = [&](int kk, int j) {
    const unsigned a = ((j & 1) ? wa1 : wa0) + (j >> 1) * 8192 + kk * 4096;
    wf[j] = cat44(lds_tr(a), lds_tr(a + 512));
  };
  auto mm = [&](int kk, int j) {
#pragma unroll
    for (int i = 0; i < 4; ++i) {
      if (j < 4) acc[i][j] = MFMA16(wf[j], xf[kk][i], acc[i][j]);
      else acc[i][j] = MFMA16(xf[kk][i], wf[j], acc[i][j]);
    }
  };
#define ILV4() do { SGB(0x8, 1); SGB(0x100, 1); SGB(0x8, 1); SGB(0x100, 1); SGB(0x8, 1); SGB(0x100, 1); \
    SGB(0x8, 1); SGB(0x100, 1); SBAR(); } while (0)
  BARRIER();
  STAMP(0, 1);
  STAMPS(0, 0);
#pragma unroll
  for (int j = 0; j < 5; ++j) ldw(0, j);
#pragma unroll
  for (int i = 0; i < 4; ++i) ldx(0, i);
  unsigned so = 0;
  SBAR();
#define K1_HEAD() do { mm(0, 0); ldw(0, 5); ILV4(); \
    mm(0, 1); ldw(1, 0); ldx(1, 0); ILV4(); \
    mm(0, 2); ldw(1, 1); ldx(1, 1); ILV4(); \
    mm(0, 3); ldw(1, 2); ldx(1, 2); ILV4(); \
    mm(0, 4); ldw(1, 3); ldx(1, 3); ILV4(); \
    mm(0, 5); ldw(1, 4); ILV4(); \
    mm(1, 0); ldw(1, 5); ILV4(); \
    mm(1, 1); SBAR(); } while (0)
  K1_HEAD();
  for (int s = 0; s < 15; ++s) {
    BARRIER_L();
    STAMPS(0, s + 1);
    {
      const unsigned d = (so == 3 * K1_STAGE) ? (unsigned)(-3 * K1_STAGE) : (unsigned)K1_STAGE;
      so += d; xa0 += d; xa1 += d; wa0 += d; wa1 += d;
    }
    SBAR();
    mm(1, 2); ldw(0, 0); ldx(0, 0); ldx(0, 1); ILV4();
    mm(1, 3); ldw(0, 1); ldx(0, 2); ldx(0, 3); ILV4();
    mm(1, 4); ldw(0, 2); ldw(0, 3); ILV4();
    mm(1, 5); ldw(0, 4); ILV4();
    K1_HEAD();
  }
  mm(1, 2); mm(1, 3); mm(1, 4); mm(1, 5);
  STAMP(0, 2);
  const int head = nt * 2 + wn;
  float kn = 0.f;
  const int fo = (g >> 1) * 1024 + (g & 1) * 512;
#pragma unroll
  for (int i = 0; i < 4; ++i) {
    const int tb = mt * 4 + wm * 2 + (i >> 1), u = i & 1;
    char* qd = ws + WS_Q + ((size_t)(head * 64 + tb) * 2) * 1024 + fo;
    char* kvd = ws + WS_KV + ((size_t)(head * 64 + tb) * 4) * 1024 + fo;
    const u32x4 qp = {pk2(acc[i][0][0] * QSCALE, acc[i][0][1] * QSCALE), pk2(acc[i][0][2] * QSCALE, acc[i][0][3] * QSCALE),
                      pk2(acc[i][1][0] * QSCALE, acc[i][1][1] * QSCALE), pk2(acc[i][1][2] * QSCALE, acc[i][1][3] * QSCALE)};
    st16(qd + (16 * u + c16) * 16, qp);
    const u32x4 kp = {pk2(acc[i][2][0], acc[i][2][1]), pk2(acc[i][2][2], acc[i][2][3]),
                      pk2(acc[i][3][0], acc[i][3][1]), pk2(acc[i][3][2], acc[i][3][3])};
    st16(kvd + ((c16 & 7) + 8 * u + 16 * (c16 >> 3)) * 16, kp);
    float ss = 0.f;
#pragma unroll
    for (int e = 0; e < 4; ++e) { const float a = bflo(kp[e]), b = bfhi(kp[e]); ss += a * a + b * b; }
    ss += __shfl_xor(ss, 16);
    kn = fmaxf(kn, xhalf_sum(ss));
    const f32x4 va = acc[i & 2][4 + u], vb = acc[(i & 2) + 1][4 + u];
    const u32x4 vp = {pk2(va[0], va[1]), pk2(va[2], va[3]), pk2(vb[0], vb[1]), pk2(vb[2], vb[3])};
    st16(kvd + 2048 + (16 * u + c16) * 16, vp);
  }
#pragma unroll
  for (int o = 1; o < 16; o <<= 1) kn = fmaxf(kn, __shfl_xor(kn, o));
  if (lane == 0) ((float*)(ws + WS_KMAX))[head * 32 + mt * 2 + wm] = kn;
  STAMP(0, 3);
}

constexpr int K2_STAGE = 16384, K2_NST = 4;
constexpr int CV_URE = 0, CV_URO = 4160, CV_HP = 8320, CV_Z = 12928, CV_SZ = 17024;
constexpr int K2_CV = K2_NST * K2_STAGE, K2_LDS = K2_CV + 4 * CV_SZ;

DI void attn_tile(const bf16x8 k0, const bf16x8 k1, const bf16x8 v0, const bf16x8 v1, const bf16x8 q0, const bf16x8 q1,
                  const f32x16& negm, const bf16x8 ones, f32x16& o, f32x4& l) {
  f32x16 s = MFMA32(k0, q0, negm);
  s = MFMA32(k1, q1, s);
#pragma unroll
  for (int r = 0; r < 16; ++r) s[r] = __builtin_amdgcn_exp2f(s[r]);
  const bf16x8 p0 = pack8n(s, 0), p1 = pack8n(s, 1);
  o = MFMA32(v0, p0, o);
  o = MFMA32(v1, p1, o);
  l = MFMA16(ones, p0, l);
  l = MFMA16(ones, p1, l);
}

__global__ __launch_bounds__(768, 3) void k2_attn_conv(const unsigned short* aq, const unsigned short* ssk,
                                                       const unsigned short* ssd, char* ws, unsigned short* out) {
  extern __shared__ __attribute__((aligned(16))) char lds[];
  const unsigned lds0 = (unsigned)(uintptr_t)(LAS char*)lds;
  const int tid = threadIdx.x, lane = tid & 63;
  const int wid = __builtin_amdgcn_readfirstlane(tid >> 6);
  const int bx = blockIdx.x, xg = bx & 7, li = bx >> 3;
  const int head = xg * 4 + (li >> 3), qb = li & 7;
  const char* kvg = ws + WS_KV + (size_t)head * 262144;
  const unsigned vl = lane * 16;
  STAMP(1, 0);
  if (wid >= 8) {
    const int cw = wid - 8;
    const int chg = xg * 32 + li;
    const int ch = chg * 4 + cw;
    const unsigned cb = lds0 + K2_CV + cw * CV_SZ;
    {
      const char* urg = ws + WS_UR + (size_t)ch * 4096;
      const char* hg = (const char*)(ssk + (size_t)ch * 2048);
#pragma unroll
      for (int i = 0; i < 4; ++i) glds16(urg + i * 1024, vl, cb + CV_URE + i * 1024);
#pragma unroll
      for (int i = 0; i < 4; ++i) glds16(hg + i * 1024, vl, cb + CV_HP + 480 + i * 1024);
      if (lane < 30) *(LAS u32x4*)(uintptr_t)(cb + CV_HP + lane * 16) = (u32x4){0u, 0u, 0u, 0u};
      if (lane < 4) *(LAS u32x4*)(uintptr_t)(cb + CV_URE + 4096 + lane * 16) = (u32x4){0u, 0u, 0u, 0u};
      if (lane < 4) *(LAS u32x4*)(uintptr_t)(cb + CV_URO + 4096 + lane * 16) = (u32x4){0u, 0u, 0u, 0u};
    }
    BARRIER();
    BARRIER();
    BARRIER();
    WAITV(0);
#pragma unroll
    for (int i = 0; i < 4; ++i) {
      const unsigned a = cb + CV_URE + i * 1024 + lane * 16;
      const u32x4 d = *(const LAS u32x4*)(uintptr_t)a;
      const unsigned nx = *(const LAS unsigned*)(uintptr_t)(a + 16);
      u32x4 o;
      o[0] = __builtin_amdgcn_alignbit(d[1], d[0], 16);
      o[1] = __builtin_amdgcn_alignbit(d[2], d[1], 16);
      o[2] = __builtin_amdgcn_alignbit(d[3], d[2], 16);
      o[3] = __builtin_amdgcn_alignbit(nx, d[3], 16);
      *(LAS u32x4*)(uintptr_t)(cb + CV_URO + i * 1024 + lane * 16) = o;
    }
    BARRIER();
    f32x4 y[8];
#pragma unroll
    for (int n = 0; n < 8; ++n) y[n] = (f32x4){};
    const int i16 = lane & 15, g = lane >> 4;
    const unsigned ua = cb + ((i16 & 1) ? CV_URE : CV_URO) + (903 - (i16 >> 1) + 4 * g) * 4;
    const unsigned ha = cb + CV_HP + i16 * 32 + g * 16;
    for (int rho = 0; rho < 8; ++rho) {
      bf16x8 uf[8], hf[8];
#pragma unroll
      for (int d = 0; d < 8; ++d) {
        const unsigned a = ua - (unsigned)(8 * d - rho) * 64;
        u32x4 t;
#pragma unroll
        for (int j = 0; j < 4; ++j) t[j] = *(const LAS unsigned*)(uintptr_t)(a + j * 4);
        uf[d] = __builtin_bit_cast(bf16x8, t);
        hf[d] = lds_rd128(ha + (8 * d + rho) * 64);
      }
#pragma unroll
      for (int n = 0; n < 8; ++n)
#pragma unroll
        for (int p = 0; p <= n; ++p) y[n] = MFMA16(uf[n - p], hf[p], y[n]);
      BARRIER();
    }
#pragma unroll
    for (int n = 0; n < 8; ++n) {
      u32x2 o = {pk2(y[n][0], y[n][1]), pk2(y[n][2], y[n][3])};
      *(LAS u32x2*)(uintptr_t)(cb + CV_Z + (256 * n + 16 * i16 + 4 * g) * 2) = o;
    }
    u32x2 qv[2][4];
#pragma unroll
    for (int h = 0; h < 2; ++h)
#pragma unroll
      for (int r = 0; r < 4; ++r)
        qv[h][r] = *(const u32x2*)(aq + (size_t)((h * 256 + cw * 64 + lane) * 4 + r) * 1024 + chg * 4);
    const u32x2 dd = *(const u32x2*)(ssd + chg * 4);
    BARRIER_L();
    BARRIER();
    BARRIER();
    STAMP(1, 1);
    const float dv[4] = {bflo(dd[0]), bfhi(dd[0]), bflo(dd[1]), bfhi(dd[1])};
#pragma unroll
    for (int h = 0; h < 2; ++h) {
      const int t0 = (h * 256 + cw * 64 + lane) * 4;
      float z[4][4];
#pragma unroll
      for (int c = 0; c < 4; ++c) {
        const unsigned cbc = lds0 + K2_CV + c * CV_SZ;
        const u32x2 yy = *(const LAS u32x2*)(uintptr_t)(cbc + CV_Z + t0 * 2);
        const u32x2 uu = *(const LAS u32x2*)(uintptr_t)(cbc + CV_URE + (2044 - t0) * 2);
        const float yv[4] = {bflo(yy[0]), bfhi(yy[0]), bflo(yy[1]), bfhi(yy[1])};
        const float uv[4] = {bfhi(uu[1]), bflo(uu[1]), bfhi(uu[0]), bflo(uu[0])};
#pragma unroll
        for (int r = 0; r < 4; ++r) z[r][c] = rbf(yv[r] + rbf(uv[r] * dv[c]));
      }
#pragma unroll
      for (int r = 0; r < 4; ++r) {
        u32x2 o = {pk2(bflo(qv[h][r][0]) * z[r][0], bfhi(qv[h][r][0]) * z[r][1]),
                   pk2(bflo(qv[h][r][1]) * z[r][2], bfhi(qv[h][r][1]) * z[r][3])};
        *(u32x2*)(out + (size_t)(t0 + r) * 2048 + chg * 4) = o;
      }
    }
    STAMP(1, 2);
    return;
  }
  auto issue = [&](int s) {
    const unsigned dst = lds0 + (s & 3) * K2_STAGE + wid * 2048;
    glds16(kvg + (size_t)s * K2_STAGE + wid * 2048, vl, dst);
    glds16(kvg + (size_t)s * K2_STAGE + wid * 2048 + 1024, vl, dst + 1024);
  };
  issue(0); issue(1);
  const int tb = qb * 8 + wid;
  const char* qg = ws + WS_Q + ((size_t)(head * 64 + tb) * 2) * 1024 + lane * 16;
  const bf16x8 q0 = *(const bf16x8*)qg, q1 = *(const bf16x8*)(qg + 1024);
  float km = ((const float*)(ws + WS_KMAX))[head * 32 + (lane & 31)];
#pragma unroll
  for (int o = 1; o < 32; o <<= 1) km = fmaxf(km, __shfl_xor(km, o));
  float qs = 0.f;
  {
    const u32x4 a = __builtin_bit_cast(u32x4, q0), b = __builtin_bit_cast(u32x4, q1);
#pragma unroll
    for (int e = 0; e < 4; ++e) {
      qs += bflo(a[e]) * bflo(a[e]) + bfhi(a[e]) * bfhi(a[e]);
      qs += bflo(b[e]) * bflo(b[e]) + bfhi(b[e]) * bfhi(b[e]);
    }
    qs = xhalf_sum(qs);
  }
  float mb = sqrtf(qs * km) * 1.0009765625f;
  asm volatile("" : "+v"(mb));
  issue(2); issue(3);
  f32x16 negm;
#pragma unroll
  for (int r = 0; r < 16; ++r) negm[r] = -mb;
  asm volatile("" : "+v"(negm));
  bf16x8 ones;
  {
    const short one = (((lane & 15) == ((lane >> 4) & 1))) ? (short)0x3f80 : (short)0;
#pragma unroll
    for (int e = 0; e < 8; ++e) ones[e] = one;
  }
  f32x16 o = (f32x16){};
  f32x4 l = (f32x4){};
  WAITV(4); BARRIER();
  STAMP(1, 1);
  const unsigned lb = lds0 + lane * 16;
  bf16x8 k0 = lds_rd128(lb), k1 = lds_rd128(lb + 1024), v0 = lds_rd128(lb + 2048), v1;
  f32x16 sA = MFMA32(k0, q0, negm), sB;
  sA = MFMA32(k1, q1, sA);
  k0 = lds_rd128(lb + 4096); k1 = lds_rd128(lb + 4096 + 1024);
  u32x4 p0, p1 = (u32x4){0u, 0u, 0u, 0u};
  v1 = ones;
#define EXP4(S, i) do { S[i] = __builtin_amdgcn_exp2f(S[i]); S[i + 1] = __builtin_amdgcn_exp2f(S[i + 1]); \
    S[i + 2] = __builtin_amdgcn_exp2f(S[i + 2]); S[i + 3] = __builtin_amdgcn_exp2f(S[i + 3]); } while (0)
#define EXP2(S, i) do { S[i] = __builtin_amdgcn_exp2f(S[i]); S[i + 1] = __builtin_amdgcn_exp2f(S[i + 1]); } while (0)
#define PK4(P, S, i) do { P[0] = pk2(S[i], S[i + 1]); P[1] = pk2(S[i + 2], S[i + 3]); P[2] = pk2(S[i + 4], S[i + 5]); \
    P[3] = pk2(S[i + 6], S[i + 7]); } while (0)
#define BF(P) __builtin_bit_cast(bf16x8, P)
#define PIN(x) asm volatile("" : "+v"(x))
#define TILE(SC, SN, a0, a1, a2) do { \
    SN = MFMA32(k0, q0, negm); k0 = lds_rd128(a2); EXP4(SC, 0); PIN(SC); SBAR(); \
    o = MFMA32(v1, BF(p1), o); v1 = lds_rd128((a0) + 3072); EXP4(SC, 4); PIN(SC); SBAR(); \
    SN = MFMA32(k1, q1, SN); k1 = lds_rd128((a2) + 1024); PK4(p0, SC, 0); EXP2(SC, 8); PIN(SC); PIN(p0); SBAR(); \
    l = MFMA16(ones, BF(p1), l); EXP2(SC, 10); PIN(SC); SBAR(); \
    o = MFMA32(v0, BF(p0), o); v0 = lds_rd128((a1) + 2048); EXP4(SC, 12); PIN(SC); SBAR(); \
    l = MFMA16(ones, BF(p0), l); PK4(p1, SC, 8); PIN(p1); SBAR(); \
  } while (0)
  SBAR();
  for (int s = 0; s < 16; ++s) {
    const unsigned a = lb + (s & 3) * K2_STAGE, an = lb + ((s + 1) & 3) * K2_STAGE;
    TILE(sA, sB, a, a + 4096, a + 8192);
    TILE(sB, sA, a + 4096, a + 8192, a + 12288);
    TILE(sA, sB, a + 8192, a + 12288, an);
    TILE(sB, sA, a + 12288, an, an + 4096);
    if (s < 13) { WAITV(2); BARRIER_L(); if (s < 12) issue(s + 4); }
    else if (s == 13) { WAITV(0); BARRIER_L(); }
    SBAR();
  }
  o = MFMA32(v1, BF(p1), o);
  l = MFMA16(ones, BF(p1), l);
  STAMP(1, 2);
  float lq;
  {
    const float a = __shfl(l[0], lane & 15), b = __shfl(l[1], lane & 15);
    lq = (lane & 16) ? b : a;
  }
  if (__builtin_expect(!__all(lq >= 9.094947017729282e-13f && lq < 3.0e38f), 0)) {
    float mx = -3.0e38f;
    const f32x16 zero = (f32x16){};
    const char* kp = kvg + lane * 16;
    for (int kb = 0; kb < 64; ++kb) {
      f32x16 s = MFMA32(*(const bf16x8*)(kp + kb * 4096), q0, zero);
      s = MFMA32(*(const bf16x8*)(kp + kb * 4096 + 1024), q1, s);
#pragma unroll
      for (int r = 0; r < 16; ++r) mx = fmaxf(mx, s[r]);
    }
    mx = xhalf_max(mx);
#pragma unroll
    for (int r = 0; r < 16; ++r) negm[r] = -mx;
    o = (f32x16){};
    l = (f32x4){};
    for (int kb = 0; kb < 64; ++kb) {
      const char* a = kp + kb * 4096;
      attn_tile(*(const bf16x8*)a, *(const bf16x8*)(a + 1024), *(const bf16x8*)(a + 2048), *(const bf16x8*)(a + 3072),
                q0, q1, negm, ones, o, l);
    }
    const float a = __shfl(l[0], lane & 15), b = __shfl(l[1], lane & 15);
    lq = (lane & 16) ? b : a;
  }
  const float inv = 1.0f / lq;
  char* od = ws + WS_O + ((size_t)(tb * 64 + head * 2)) * 1024 + lane * 16;
  *(bf16x8*)od = pack8(o, 0, inv);
  *(bf16x8*)(od + 1024) = pack8(o, 1, inv);
  STAMP(1, 3);
}

constexpr int K3_LDS = 131072;
__global__ __launch_bounds__(512, 2) void k3_oproj(const unsigned short* wo, char* ws, unsigned short* out) {
  extern __shared__ __attribute__((aligned(16))) char lds[];
  const unsigned lds0 = (unsigned)(uintptr_t)(LAS char*)lds;
  const int tid = threadIdx.x, lane = tid & 63;
  const int wid = __builtin_amdgcn_readfirstlane(tid >> 6);
  const int bx = blockIdx.x, xg = bx & 7, li = bx >> 3;
  const int mt = 4 * (xg & 3) + (li & 3), nt = 8 * (xg >> 2) + (li >> 2);
  STAMP(2, 0);
  if (wid >= 4) {
    const int lw = wid - 4;
    const unsigned vw = ((lane >> 3) * 1024 + (((lane & 7) ^ (((lane >> 4) & 1) << 2)) * 8)) * 2;
    const char* wb = (const char*)(wo + nt * 64);
#pragma unroll
    for (int c = 0; c < 8; ++c)
#pragma unroll
      for (int i = 0; i < 4; ++i) {
        const int p = c * 16 + lw * 4 + i;
        glds16(wb + (size_t)p * 8 * 2048, vw, lds0 + p * 1024);
      }
    WAITV(28); BARRIER(); WAITV(24); BARRIER(); WAITV(20); BARRIER(); WAITV(16); BARRIER();
    WAITV(12); BARRIER(); WAITV(8); BARRIER(); WAITV(4); BARRIER(); WAITV(0); BARRIER();
    STAMP(2, 1);
    return;
  }
  const int tb = mt * 4 + wid;
  const char* og = ws + WS_O + (size_t)tb * 65536 + lane * 16;
  const int hi = lane >> 5, q4 = (lane & 15) >> 2;
  const unsigned wa0 = lds0 + (hi * 4 + q4) * 128 + ((0 ^ (q4 >> 1)) * 64) + ((lane >> 4) & 1) * 32 + (lane & 3) * 8;
  const unsigned wa1 = lds0 + (hi * 4 + q4) * 128 + ((1 ^ (q4 >> 1)) * 64) + ((lane >> 4) & 1) * 32 + (lane & 3) * 8;
  f32x16 acc0 = (f32x16){}, acc1 = (f32x16){};
  constexpr int OD = 24;
  bf16x8 of[OD];
#pragma unroll
  for (int j = 0; j < OD; ++j) of[j] = *(const bf16x8*)(og + j * 1024);
  bf16x8 w0[4], w1[4];
  auto ldw = [&](int j) {
    const unsigned ko = j * 2048;
    w0[j & 3] = cat44(lds_tr(wa0 + ko), lds_tr(wa0 + ko + 1024));
    w1[j & 3] = cat44(lds_tr(wa1 + ko), lds_tr(wa1 + ko + 1024));
  };
  BARRIER();
  STAMP(2, 1);
  ldw(0); ldw(1); ldw(2);
  SBAR();
#pragma unroll
  for (int j = 0; j < 64; ++j) {
    if (((j + 3) & 7) == 0 && j + 3 < 64) BARRIER();
    if (j + 3 < 64) ldw(j + 3);
    acc0 = MFMA32(w0[j & 3], of[j % OD], acc0);
    acc1 = MFMA32(w1[j & 3], of[j % OD], acc1);
    if (j + OD < 64) of[j % OD] = *(const bf16x8*)(og + (j + OD) * 1024);
    SBAR();
  }
  STAMP(2, 2);
  unsigned short* od = out + (size_t)(tb * 32 + (lane & 31)) * 2048 + 1024 + nt * 64 + hi * 4;
#pragma unroll
  for (int gq = 0; gq < 4; ++gq) {
    u32x2 a = {pk2(acc0[4 * gq], acc0[4 * gq + 1]), pk2(acc0[4 * gq + 2], acc0[4 * gq + 3])};
    u32x2 b = {pk2(acc1[4 * gq], acc1[4 * gq + 1]), pk2(acc1[4 * gq + 2], acc1[4 * gq + 3])};
    *(u32x2*)(od + gq * 8) = a;
    *(u32x2*)(od + 32 + gq * 8) = b;
  }
  STAMP(2, 3);
}

extern "C" void kernel_launch(void* const* d_in, const int* in_sizes, int n_in, void* d_out, int out_size,
                              void* d_ws, size_t ws_size, hipStream_t stream) {
  const unsigned short* aq = (const unsigned short*)d_in[0];
  const unsigned short* ak = (const unsigned short*)d_in[1];
  const unsigned short* av = (const unsigned short*)d_in[2];
  const unsigned short* shk = (const unsigned short*)d_in[3];
  const unsigned short* ssk = (const unsigned short*)d_in[4];
  const unsigned short* ssd = (const unsigned short*)d_in[5];
  const unsigned short* x = (const unsigned short*)d_in[6];
  const unsigned short* wqkv = (const unsigned short*)d_in[7];
  const unsigned short* wo = (const unsigned short*)d_in[8];
  char* ws = (char*)d_ws;
  unsigned short* out = (unsigned short*)d_out;
  (void)hipFuncSetAttribute((const void*)k1_qkv, hipFuncAttributeMaxDynamicSharedMemorySize, K1_LDS);
  (void)hipFuncSetAttribute((const void*)k2_attn_conv, hipFuncAttributeMaxDynamicSharedMemorySize, K2_LDS);
  (void)hipFuncSetAttribute((const void*)k3_oproj, hipFuncAttributeMaxDynamicSharedMemorySize, K3_LDS);
  k1_qkv<<<256, 768, K1_LDS, stream>>>(x, wqkv, ak, av, shk, ws);
  k2_attn_conv<<<256, 768, K2_LDS, stream>>>(aq, ssk, ssd, ws, out);
  k3_oproj<<<256, 512, K3_LDS, stream>>>(wo, ws, out);
}
```

```cpp
#include <hip/hip_runtime.h>
#include <hip/hip_bf16.h>
#include <stdint.h>

typedef short bf16x8 __attribute__((ext_vector_type(8)));
typedef short s16x4 __attribute__((ext_vector_type(4)));
typedef float f32x16 __attribute__((ext_vector_type(16)));
typedef float f32x4 __attribute__((ext_vector_type(4)));
typedef unsigned u32x4 __attribute__((ext_vector_type(4)));
typedef unsigned u32x2 __attribute__((ext_vector_type(2)));
typedef __bf16 bf16v2 __attribute__((ext_vector_type(2)));
typedef float f32x2 __attribute__((ext_vector_type(2)));
#define LAS __attribute__((address_space(3)))
#define DI __device__ __forceinline__
#define MFMA32(a, b, c) __builtin_amdgcn_mfma_f32_32x32x16_bf16((a), (b), (c), 0, 0, 0)
#define MFMA16(a, b, c) __builtin_amdgcn_mfma_f32_16x16x32_bf16((a), (b), (c), 0, 0, 0)

constexpr int S_ = 2048, D_ = 1024, NH = 32;
constexpr size_t WS_Q = 0;
constexpr size_t WS_KV = WS_Q + (4u << 20);
constexpr size_t WS_O = WS_KV + (8u << 20);
constexpr size_t WS_UR = WS_O + (4u << 20);
constexpr size_t WS_KMAX = WS_UR + (4u << 20);
constexpr size_t WS_CNT = WS_KMAX + 4096;
#define CONV_START 8

DI unsigned pk2(float lo, float hi) {
  f32x2 v = {lo, hi};
  bf16v2 b = __builtin_convertvector(v, bf16v2);
  return __builtin_bit_cast(unsigned, b);
}
DI float bflo(unsigned u) { return __uint_as_float(u << 16); }
DI float bfhi(unsigned u) { return __uint_as_float(u & 0xffff0000u); }
DI float rbf(float x) { return bflo(pk2(x, x)); }
DI bf16x8 pack8(const f32x16& x, int s, float scale) {
  u32x4 p;
  p[0] = pk2(x[8 * s + 0] * scale, x[8 * s + 1] * scale);
  p[1] = pk2(x[8 * s + 2] * scale, x[8 * s + 3] * scale);
  p[2] = pk2(x[8 * s + 4] * scale, x[8 * s + 5] * scale);
  p[3] = pk2(x[8 * s + 6] * scale, x[8 * s + 7] * scale);
  return __builtin_bit_cast(bf16x8, p);
}
DI bf16x8 pack8n(const f32x16& x, int s) {
  u32x4 p;
  p[0] = pk2(x[8 * s + 0], x[8 * s + 1]);
  p[1] = pk2(x[8 * s + 2], x[8 * s + 3]);
  p[2] = pk2(x[8 * s + 4], x[8 * s + 5]);
  p[3] = pk2(x[8 * s + 6], x[8 * s + 7]);
  return __builtin_bit_cast(bf16x8, p);
}
DI void glds16(const void* gbase, unsigned voff, unsigned lds_dst) {
  unsigned keep;
  asm volatile("s_mov_b32 %0, m0\n\ts_mov_b32 m0, %3\n\ts_nop 0\n\tglobal_load_lds_dwordx4 %1, %2\n\ts_mov_b32 m0, %0"
               : "=&s"(keep) : "v"(voff), "s"(gbase), "s"(lds_dst) : "memory");
}
DI bf16x8 lds_rd128(unsigned a) { return *(const LAS bf16x8*)(uintptr_t)a; }
DI s16x4 lds_tr(unsigned a) {
  typedef short v4i16_t __attribute__((ext_vector_type(4)));
  return __builtin_amdgcn_ds_read_tr16_b64_v4i16((LAS v4i16_t*)(uintptr_t)a);
}
DI bf16x8 cat44(s16x4 lo, s16x4 hi) { return __builtin_shufflevector(lo, hi, 0, 1, 2, 3, 4, 5, 6, 7); }
#define SBAR() __builtin_amdgcn_sched_barrier(0)
#define SGB(m, n) __builtin_amdgcn_sched_group_barrier((m), (n), 0)
#define WAITV(n) asm volatile("s_waitcnt vmcnt(" #n ")" ::: "memory")
#define BARRIER_L() asm volatile("s_waitcnt lgkmcnt(0)\n\ts_barrier" ::: "memory")
#define BARRIER() asm volatile("s_barrier" ::: "memory")
#define STR1(i) do { } while (0)
#define STR(i) do { } while (0)
#define STAMP(k, i) do { } while (0)
#define STAMPS(k, i) do { } while (0)
DI float xhalf_sum(float v) {
  auto r = __builtin_amdgcn_permlane32_swap(__float_as_uint(v), __float_as_uint(v), false, false);
  return __uint_as_float(r[0]) + __uint_as_float(r[1]);
}
DI float xhalf_max(float v) {
  auto r = __builtin_amdgcn_permlane32_swap(__float_as_uint(v), __float_as_uint(v), false, false);
  return fmaxf(__uint_as_float(r[0]), __uint_as_float(r[1]));
}

constexpr int K1_STAGE = 40960, K1_LDS = 4 * K1_STAGE;
constexpr float QSCALE = 0.17677669529663687f * 1.4426950408889634f;
#define K1_MAP 0
#define K1_STAG 2
#define K1_PF 1
#define K1_PRIO 0
#define K1_ST 2
DI void st16(void* p, u32x4 v) {
  asm volatile("global_store_dwordx4 %0, %1, off sc1\n\ts_nop 1" :: "v"(p), "v"(v) : "memory");
}

__global__ __launch_bounds__(768, 3) void k1_qkv(const unsigned short* x, const unsigned short* w,
                                                 const unsigned short* ak, const unsigned short* av,
                                                 const unsigned short* shk, char* ws) {
  extern __shared__ __attribute__((aligned(16))) char lds[];
  const unsigned lds0 = (unsigned)(uintptr_t)(LAS char*)lds;
  const int tid = threadIdx.x, lane = tid & 63;
  const int wid = __builtin_amdgcn_readfirstlane(tid >> 6);
  const int bx = blockIdx.x, xg = bx & 7, li = bx >> 3;
  const int mt = 8 * (xg & 1) + (li & 7), nt = 4 * (xg >> 1) + (li >> 3);
  STAMP(0, 0);
  STR1(0);
#define K1_S0 ((li & 7) + (li >> 3))
  const int s0 = K1_S0;
  if (wid >= 8) {
    const int lt = tid - 512;
    const int cg = lt & 7, tg = lt >> 3;
    const int ch0 = (bx & 15) * 64 + cg * 8, t0 = (bx >> 4) * 128 + tg * 4;
    u32x4 kr[7], vr[4], wk[4];
    unsigned short* ur = (unsigned short*)(ws + WS_UR);
    if (bx == 0 && lt < 8) ((unsigned*)(ws + WS_CNT))[lt * 64] = 0u;
    auto ldk = [&](int r) { const int t = t0 - 3 + r; kr[r] = *(const u32x4*)(ak + (size_t)(t < 0 ? 0 : t) * 1024 + ch0); };
    auto ldv = [&](int r) { vr[r] = *(const u32x4*)(av + (size_t)(t0 + r) * 1024 + ch0); };
    auto ldw = [&](int r) { wk[r] = *(const u32x4*)(shk + (size_t)ch0 * 4 + r * 8); };
#define PL0 11
#define PLN 6
#define PC0 15
#define PCN 8
    auto ld = [&](int i) { if (i < 7) ldk(i); else if (i < 11) ldv(i - 7); };
    ldw(0); ldw(1); ldw(2); ldw(3);
    auto cmp = [&](int c) {
      const unsigned wa = wk[c >> 1][(c & 1) * 2], wb2 = wk[c >> 1][(c & 1) * 2 + 1];
      const float w0 = bflo(wa), w1 = bfhi(wa), w2 = bflo(wb2), w3 = bfhi(wb2);
      float kf[7], u[4];
#pragma unroll
      for (int r = 0; r < 7; ++r) kf[r] = (c & 1) ? bfhi(kr[r][c >> 1]) : bflo(kr[r][c >> 1]);
#pragma unroll
      for (int r = 0; r < 4; ++r) {
        const float a = fmaf(kf[r + 3], w0, fmaf(kf[r + 2], w1, fmaf(kf[r + 1], w2, kf[r] * w3)));
        const float vv = (c & 1) ? bfhi(vr[r][c >> 1]) : bflo(vr[r][c >> 1]);
        u[r] = a * vv;
      }
      u32x2 o = {pk2(u[3], u[2]), pk2(u[1], u[0])};
      *(u32x2*)(ur + (size_t)(ch0 + c) * 2048 + (2044 - t0)) = o;
    };
#pragma unroll
    for (int b = 0; b < 16; ++b) {
      SBAR(); BARRIER(); SBAR();
#pragma unroll
      for (int i = 0; i < PLN; ++i) if (b >= PL0) ld((b - PL0) * PLN + i);
      if (b == PC0 && t0 == 0) { kr[0] = (u32x4){0u, 0u, 0u, 0u}; kr[1] = kr[0]; kr[2] = kr[0]; }
#pragma unroll
      for (int c = 0; c < 8; ++c) if (b >= PC0 && (c / PCN == b - PC0 || (b == 15 && c / PCN > 15 - PC0))) cmp(c);
      if (b >= PC0 && (8 + PCN - 1) / PCN - 1 <= b - PC0) break;
    }
    STAMP(0, 2);
    return;
  }
  if (wid >= 4) {
    const int lw = wid - 4;
    const unsigned rowl = lane >> 3, sl = lane & 7;
    unsigned vx[4], vw[6];
#pragma unroll
    for (int i = 0; i < 4; ++i)
      vx[i] = (i * 8 + rowl) * 2048 + ((sl ^ (((i & 1) * 4 + (lane >> 4)) & 7)) * 16) - i * 1024;
#pragma unroll
    for (int i = 0; i < 6; ++i) {
      const int q = lw * 6 + i, sec = q >> 3, p = q & 7;
      vw[i] = (p * 8 + rowl) * 6144 + sec * 2048 + ((sl ^ (((lane >> 4) & 1) << 1) ^ ((i & 1) << 2)) * 16) - (i % 3) * 1024;
    }
    const char* xb = (const char*)(x + (size_t)(mt * 128 + lw * 32) * 1024);
    const char* wb = (const char*)(w + nt * 64);
    auto issue = [&](int s, int buf) {
#define K1_WIN 4
      const int ks = (s & (16 - K1_WIN)) | ((s + s0) & (K1_WIN - 1));
      const unsigned dst = lds0 + buf * K1_STAGE;
      const char* xs = xb + ks * 128;
      const char* wsrc = wb + (size_t)ks * 64 * 6144;
      asm volatile("s_mov_b32 m0, %0\n\ts_nop 0\n\t"
                   "global_load_lds_dwordx4 %3, %1\n\tglobal_load_lds_dwordx4 %4, %1 offset:1024\n\t"
                   "global_load_lds_dwordx4 %5, %1 offset:2048\n\tglobal_load_lds_dwordx4 %6, %1 offset:3072\n\t"
                   "s_mov_b32 m0, %13\n\ts_nop 0\n\t"
                   "global_load_lds_dwordx4 %7, %2\n\tglobal_load_lds_dwordx4 %8, %2 offset:1024\n\t"
                   "global_load_lds_dwordx4 %9, %2 offset:2048\n\t"
                   "s_mov_b32 m0, %14\n\ts_nop 0\n\t"
                   "global_load_lds_dwordx4 %10, %2\n\tglobal_load_lds_dwordx4 %11, %2 offset:1024\n\t"
                   "global_load_lds_dwordx4 %12, %2 offset:2048"
                   :: "s"(dst + lw * 4096), "s"(xs), "s"(wsrc), "v"(vx[0]), "v"(vx[1]), "v"(vx[2]), "v"(vx[3]),
                      "v"(vw[0]), "v"(vw[1]), "v"(vw[2]), "v"(vw[3]), "v"(vw[4]), "v"(vw[5]),
                      "s"(dst + 16384 + lw * 6144), "s"(dst + 16384 + lw * 6144 + 3072) : "memory");
    };
    STR1(1);
    issue(0, 0); issue(1, 1);
    STR1(2);
    WAITV(11);
    STR1(3);
    WAITV(10); BARRIER();
    STR1(4);
    issue(2, 2); issue(3, 3);
    STR1(5);
    for (int s = 0; s < 12; ++s) {
      WAITV(20); BARRIER();
      issue(s + 4, s & 3);
    }
    WAITV(20); BARRIER();
    WAITV(10); BARRIER();
    WAITV(0); BARRIER();
    STAMP(0, 1);
    return;
  }
  const int wm = wid & 1, wn = wid >> 1;
  const int c16 = lane & 15, g = lane >> 4, q4 = c16 >> 2;
  const unsigned xa = lds0 + (wm * 64 + c16) * 128 + ((g ^ (c16 >> 1)) * 16);
  const unsigned wa = lds0 + 16384 + (8 * g + q4) * 128 + (((2 * wn) ^ ((q4 >> 1) | ((g & 1) << 1))) * 32) + (lane & 3) * 8;
  f32x4 acc[4][6];
#pragma unroll
  for (int i = 0; i < 4; ++i)
#pragma unroll
    for (int j = 0; j < 6; ++j) acc[i][j] = (f32x4){};
  bf16x8 xf[2][4], wf[6];
  unsigned xa0 = xa, xa1 = xa ^ 64, wa0 = wa, wa1 = wa ^ 32;
  auto ldx = [&](int kk, int i) { xf[kk][i] = lds_rd128((kk ? xa1 : xa0) + i * 2048); };
  auto ldw = [&](int kk, int j) {
    const unsigned a = ((j & 1) ? wa1 : wa0) + (j >> 1) * 8192 + kk * 4096;
    wf[j] = cat44(lds_tr(a), lds_tr(a + 512));
  };
  auto mm = [&](int kk, int j) {
#pragma unroll
    for (int i = 0; i < 4; ++i) {
      if (j < 4) acc[i][j] = MFMA16(wf[j], xf[kk][i], acc[i][j]);
      else acc[i][j] = MFMA16(xf[kk][i], wf[j], acc[i][j]);
    }
  };
#define ILV4() do { SGB(0x8, 1); SGB(0x100, 1); SGB(0x8, 1); SGB(0x100, 1); SGB(0x8, 1); SGB(0x100, 1); \
    SGB(0x8, 1); SGB(0x100, 1); SBAR(); } while (0)
  BARRIER();
  STAMP(0, 1);
  STAMPS(0, 0);
#pragma unroll
  for (int j = 0; j < 5; ++j) ldw(0, j);
#pragma unroll
  for (int i = 0; i < 4; ++i) ldx(0, i);
  unsigned so = 0;
  SBAR();
#define K1_HEAD() do { mm(0, 0); ldw(0, 5); ILV4(); \
    mm(0, 1); ldw(1, 0); ldx(1, 0); ILV4(); \
    mm(0, 2); ldw(1, 1); ldx(1, 1); ILV4(); \
    mm(0, 3); ldw(1, 2); ldx(1, 2); ILV4(); \
    mm(0, 4); ldw(1, 3); ldx(1, 3); ILV4(); \
    mm(0, 5); ldw(1, 4); ILV4(); \
    mm(1, 0); ldw(1, 5); ILV4(); \
    mm(1, 1); SBAR(); } while (0)
  K1_HEAD();
  for (int s = 0; s < 15; ++s) {
    BARRIER_L();
    STAMPS(0, s + 1);
    {
      const unsigned d = (so == 3 * K1_STAGE) ? (unsigned)(-3 * K1_STAGE) : (unsigned)K1_STAGE;
      so += d; xa0 += d; xa1 += d; wa0 += d; wa1 += d;
    }
    SBAR();
    mm(1, 2); ldw(0, 0); ldx(0, 0); ldx(0, 1); ILV4();
    mm(1, 3); ldw(0, 1); ldx(0, 2); ldx(0, 3); ILV4();
    mm(1, 4); ldw(0, 2); ldw(0, 3); ILV4();
    mm(1, 5); ldw(0, 4); ILV4();
    K1_HEAD();
  }
  mm(1, 2); mm(1, 3); mm(1, 4); mm(1, 5);
  STAMP(0, 2);
  const int head = nt * 2 + wn;
  float kn = 0.f;
  const int fo = (g >> 1) * 1024 + (g & 1) * 512;
#pragma unroll
  for (int i = 0; i < 4; ++i) {
    const int tb = mt * 4 + wm * 2 + (i >> 1), u = i & 1;
    char* qd = ws + WS_Q + ((size_t)(head * 64 + tb) * 2) * 1024 + fo;
    char* kvd = ws + WS_KV + ((size_t)(head * 64 + tb) * 4) * 1024 + fo;
    const u32x4 qp = {pk2(acc[i][0][0] * QSCALE, acc[i][0][1] * QSCALE), pk2(acc[i][0][2] * QSCALE, acc[i][0][3] * QSCALE),
                      pk2(acc[i][1][0] * QSCALE, acc[i][1][1] * QSCALE), pk2(acc[i][1][2] * QSCALE, acc[i][1][3] * QSCALE)};
    st16(qd + (16 * u + c16) * 16, qp);
    const u32x4 kp = {pk2(acc[i][2][0], acc[i][2][1]), pk2(acc[i][2][2], acc[i][2][3]),
                      pk2(acc[i][3][0], acc[i][3][1]), pk2(acc[i][3][2], acc[i][3][3])};
    st16(kvd + ((c16 & 7) + 8 * u + 16 * (c16 >> 3)) * 16, kp);
    float ss = 0.f;
#pragma unroll
    for (int e = 0; e < 4; ++e) { const float a = bflo(kp[e]), b = bfhi(kp[e]); ss += a * a + b * b; }
    ss += __shfl_xor(ss, 16);
    kn = fmaxf(kn, xhalf_sum(ss));
    const f32x4 va = acc[i & 2][4 + u], vb = acc[(i & 2) + 1][4 + u];
    const u32x4 vp = {pk2(va[0], va[1]), pk2(va[2], va[3]), pk2(vb[0], vb[1]), pk2(vb[2], vb[3])};
    st16(kvd + 2048 + (16 * u + c16) * 16, vp);
  }
#pragma unroll
  for (int o = 1; o < 16; o <<= 1) kn = fmaxf(kn, __shfl_xor(kn, o));
  if (lane == 0) ((float*)(ws + WS_KMAX))[head * 32 + mt * 2 + wm] = kn;
  STAMP(0, 3);
}

constexpr int K2_STAGE = 16384;
constexpr int K2_WE = 65536;
constexpr int K2_CV = 98304, CV_SZ = 16384;
constexpr int CV_URE = 0, CV_URO = 4160, CV_HP = 8320, CV_Z = 0;
constexpr int K2_LDS = 163840;
typedef __attribute__((address_space(1))) unsigned gu32;
#define ATT_PRIO 0

DI void glds4(const void* gbase, unsigned v0, unsigned v1, unsigned v2, unsigned v3, unsigned lds_dst) {
  asm volatile("s_mov_b32 m0, %5\n\ts_nop 0\n\t"
               "global_load_lds_dwordx4 %1, %0\n\tglobal_load_lds_dwordx4 %2, %0 offset:1024\n\t"
               "global_load_lds_dwordx4 %3, %0 offset:2048\n\tglobal_load_lds_dwordx4 %4, %0 offset:3072"
               :: "s"(gbase), "v"(v0), "v"(v1), "v"(v2), "v"(v3), "s"(lds_dst) : "memory");
}

#define STA 0
#define STB 0
DI void st8a(void* p, u32x2 v) {
  *(u32x2*)p = v;
}
DI void st16b(void* p, u32x4 v) {
  *(u32x4*)p = v;
}

DI void attn_tile(const bf16x8 k0, const bf16x8 k1, const bf16x8 v0, const bf16x8 v1, const bf16x8 q0, const bf16x8 q1,
                  const f32x16& negm, const bf16x8 ones, f32x16& o, f32x4& l) {
  f32x16 s = MFMA32(k0, q0, negm);
  s = MFMA32(k1, q1, s);
#pragma unroll
  for (int r = 0; r < 16; ++r) s[r] = __builtin_amdgcn_exp2f(s[r]);
  const bf16x8 p0 = pack8n(s, 0), p1 = pack8n(s, 1);
  o = MFMA32(v0, p0, o);
  o = MFMA32(v1, p1, o);
  l = MFMA16(ones, p0, l);
  l = MFMA16(ones, p1, l);
}

__global__ __launch_bounds__(768, 3) void k2_attn_conv(const unsigned short* aq, const unsigned short* ssk,
                                                       const unsigned short* ssd, const unsigned short* wo,
                                                       char* ws, unsigned short* out) {
  extern __shared__ __attribute__((aligned(16))) char lds[];
  const unsigned lds0 = (unsigned)(uintptr_t)(LAS char*)lds;
  const int tid = threadIdx.x, lane = tid & 63;
  const int wid = __builtin_amdgcn_readfirstlane(tid >> 6);
  const int bx = blockIdx.x, xg = bx & 7, li = bx >> 3;
  const int head = xg * 4 + (li >> 3), qb = li & 7;
  const int mt = 4 * (xg & 3) + (li & 3), nt = 8 * (xg >> 2) + (li >> 2);
  const char* kvg = ws + WS_KV + (size_t)head * 262144;
  const unsigned vl = lane * 16;
  const unsigned vwo = (lane >> 3) * 2048 + (((lane & 7) ^ (((lane >> 4) & 1) << 2)) * 16);
  const char* wob = (const char*)(wo + nt * 64);
#define OP_STAG 0
#define OP_AUX 16
#define OP_OD 20
  const int g0 = (OP_STAG & 1) ? (li >> 3) * 2 : 0, kflip = (OP_STAG & 2) ? (li >> 2) & 1 : 0;
  auto wo4 = [&](int half, int pos, unsigned dst) {
    const int row0 = half * 512 + ((pos + 4 * g0) & 31) * 16;
    glds4(wob + (size_t)row0 * 2048, vwo, vwo + 15360, vwo + 30720, vwo + 46080, dst);
  };
  STAMP(1, 0);
  STR(0);
  if (wid >= 8) {
    const int cw = wid - 8;
    const int chg = xg * 32 + li;
    const int ch = chg * 4 + cw;
    const unsigned cb = lds0 + K2_CV + cw * CV_SZ;
    auto issue = [&](int s) { glds4(kvg + (size_t)s * K2_STAGE + cw * 4096, vl, vl, vl, vl, lds0 + (s & 3) * K2_STAGE + cw * 4096); };
    u32x2 qv[8];
    auto ldq = [&](int i) {
      qv[i] = *(const u32x2*)(aq + (size_t)(((i >> 2) * 256 + cw * 64 + lane) * 4 + (i & 3)) * 1024 + chg * 4);
    };
    issue(0); issue(1);
    glds4(ws + WS_UR + (size_t)ch * 4096, vl, vl, vl, vl, cb + CV_URE);
    const unsigned dd = *(const __attribute__((address_space(4))) unsigned*)(uintptr_t)(ssd + (ch & ~1));
    if (lane < 30) *(LAS u32x4*)(uintptr_t)(cb + CV_HP + lane * 16) = (u32x4){0u, 0u, 0u, 0u};
    if (lane < 4) *(LAS u32x4*)(uintptr_t)(cb + CV_URE + 4096 + lane * 16) = (u32x4){0u, 0u, 0u, 0u};
    if (lane < 4) *(LAS u32x4*)(uintptr_t)(cb + CV_URO + 4096 + lane * 16) = (u32x4){0u, 0u, 0u, 0u};
    WAITV(4); BARRIER();
    issue(2);
    glds4(ssk + (size_t)ch * 2048, vl, vl, vl, vl, cb + CV_HP + 480);
    issue(3);
    ldq(0);
    WAITV(13);
#pragma unroll
    for (int i = 0; i < 4; ++i) {
      const unsigned a = cb + CV_URE + i * 1024 + lane * 16;
      const u32x4 d = *(const LAS u32x4*)(uintptr_t)a;
      const unsigned nx = *(const LAS unsigned*)(uintptr_t)(a + 16);
      u32x4 o;
      o[0] = __builtin_amdgcn_alignbit(d[1], d[0], 16);
      o[1] = __builtin_amdgcn_alignbit(d[2], d[1], 16);
      o[2] = __builtin_amdgcn_alignbit(d[3], d[2], 16);
      o[3] = __builtin_amdgcn_alignbit(nx, d[3], 16);
      *(LAS u32x4*)(uintptr_t)(cb + CV_URO + i * 1024 + lane * 16) = o;
    }
    const int i16 = lane & 15, g = lane >> 4;
    f32x4 y[8];
    {
      const float dv = (ch & 1) ? bfhi(dd) : bflo(dd);
#pragma unroll
      for (int n = 0; n < 8; ++n) {
        const u32x2 uu = *(const LAS u32x2*)(uintptr_t)(cb + CV_URE + (2044 - (256 * n + 16 * i16 + 4 * g)) * 2);
        y[n] = (f32x4){bfhi(uu[1]) * dv, bflo(uu[1]) * dv, bfhi(uu[0]) * dv, bflo(uu[0]) * dv};
        asm volatile("" : "+v"(y[n]));
      }
    }
    unsigned ua = cb + ((i16 & 1) ? CV_URE : CV_URO) + (903 - (i16 >> 1) + 4 * g) * 4 - 7 * 512;
    unsigned ha = cb + CV_HP + i16 * 32 + g * 16;
    bf16x8 uf[8], hf[8];
    auto ldu = [&](int d) {
      u32x4 t;
#pragma unroll
      for (int j = 0; j < 4; ++j) t[j] = *(const LAS unsigned*)(uintptr_t)(ua + (7 - d) * 512 + j * 4);
      uf[d] = __builtin_bit_cast(bf16x8, t);
    };
    auto ldh = [&](int d) { hf[d] = lds_rd128(ha + d * 512); };
#pragma unroll
    for (int d = 0; d < 8; ++d) ldu(d);
    SBAR();
    STR(1);
#pragma unroll
    for (int rho = 0; rho < 8; ++rho) {
      if (rho == 0) WAITV(5); else if (rho == 2 || rho == 4) WAITV(10); else if (rho == 3) WAITV(14); else WAITV(6);
      BARRIER(); issue(rho + 4);
      if (rho < 7) ldq(rho + 1);
      SBAR();
      if (rho == 1 || rho == 2) wo4(rho - 1, cw * 2, lds0 + K2_WE + (rho - 1) * 16384 + cw * 4096);
      if (rho == 0) {
#pragma unroll
        for (int d = 0; d < 8; ++d) ldh(7 - d);
      }
      ua += 64; ha += 64;
      SBAR();
#pragma unroll
      for (int p = 7; p >= 1; --p) {
#pragma unroll
        for (int n = p; n < 8; ++n) y[n] = MFMA16(uf[n - p], hf[p], y[n]);
        SBAR();
        if (rho < 7) ldh(p);
        SBAR();
      }
#pragma unroll
      for (int n = 0; n < 8; ++n) {
        y[n] = MFMA16(uf[n], hf[0], y[n]);
        SBAR();
        if (rho < 7) ldu(n);
        SBAR();
      }
      if (rho < 7) ldh(0);
      SBAR();
    }
    STR(2);
#pragma unroll
    for (int n = 0; n < 8; ++n) {
      const u32x2 o = {pk2(y[n][0], y[n][1]), pk2(y[n][2], y[n][3])};
      *(LAS u32x2*)(uintptr_t)(cb + CV_Z + (256 * n + 16 * i16 + 4 * g) * 2) = o;
    }
    SBAR();
#pragma unroll
    for (int i = 0; i < 8; ++i) asm volatile("" : "+v"(qv[i]));
    WAITV(0); BARRIER_L(); issue(12);
    STR(3);
    u32x2 zz[2][4];
#pragma unroll
    for (int h = 0; h < 2; ++h)
#pragma unroll
      for (int c = 0; c < 4; ++c)
        zz[h][c] = *(const LAS u32x2*)(uintptr_t)(lds0 + K2_CV + c * CV_SZ + CV_Z + (h * 256 + cw * 64 + lane) * 8);
    auto sto = [&](int i) {
      const int h = i >> 2, r = i & 3;
      float z[4];
#pragma unroll
      for (int c = 0; c < 4; ++c) z[c] = (r & 1) ? bfhi(zz[h][c][r >> 1]) : bflo(zz[h][c][r >> 1]);
      const u32x2 q = qv[i];
      const u32x2 o = {pk2(bflo(q[0]) * z[0], bfhi(q[0]) * z[1]), pk2(bflo(q[1]) * z[2], bfhi(q[1]) * z[3])};
      st8a(out + (size_t)((h * 256 + cw * 64 + lane) * 4 + r) * 2048 + chg * 4, o);
    };
    SBAR();
    sto(0); sto(1);
    SBAR();
    STR(4);
    BARRIER_L(); issue(13);
    wo4(cw >> 1, 8 + (cw & 1) * 8, cb); wo4(cw >> 1, 10 + (cw & 1) * 8, cb + 4096);
    SBAR(); sto(2); sto(3); SBAR();
    WAITV(16); BARRIER(); issue(14);
    wo4(cw >> 1, 12 + (cw & 1) * 8, cb + 8192); wo4(cw >> 1, 14 + (cw & 1) * 8, cb + 12288);
    SBAR(); sto(4); sto(5); SBAR();
    WAITV(24); BARRIER(); issue(15);
    SBAR(); sto(6); sto(7); SBAR();
    WAITV(16); BARRIER();
    wo4(0, 24 + cw * 2, lds0 + cw * 4096);
    WAITV(6); BARRIER();
    wo4(1, 24 + cw * 2, lds0 + K2_STAGE + cw * 4096);
    WAITV(0);
    STR(5);
    BARRIER();
    return;
  }
  const int tb = qb * 8 + wid;
  const char* qg = ws + WS_Q + ((size_t)(head * 64 + tb) * 2) * 1024 + lane * 16;
  const bf16x8 q0 = *(const bf16x8*)qg, q1 = *(const bf16x8*)(qg + 1024);
  float km = ((const float*)(ws + WS_KMAX))[head * 32 + (lane & 31)];
#pragma unroll
  for (int o = 1; o < 32; o <<= 1) km = fmaxf(km, __shfl_xor(km, o));
  float qs = 0.f;
  {
    const u32x4 a = __builtin_bit_cast(u32x4, q0), b = __builtin_bit_cast(u32x4, q1);
#pragma unroll
    for (int e = 0; e < 4; ++e) {
      qs += bflo(a[e]) * bflo(a[e]) + bfhi(a[e]) * bfhi(a[e]);
      qs += bflo(b[e]) * bflo(b[e]) + bfhi(b[e]) * bfhi(b[e]);
    }
    qs = xhalf_sum(qs);
  }
  const float mb = sqrtf(qs * km) * 1.0009765625f;
  f32x16 negm;
#pragma unroll
  for (int r = 0; r < 16; ++r) negm[r] = -mb;
  asm volatile("" : "+v"(negm));
  bf16x8 ones;
  {
    const short one = (((lane & 15) == ((lane >> 4) & 1))) ? (short)0x3f80 : (short)0;
#pragma unroll
    for (int e = 0; e < 8; ++e) ones[e] = one;
  }
  f32x16 o = (f32x16){};
  f32x4 l = (f32x4){};
  BARRIER();
  STAMP(1, 1);
  const unsigned lb = lds0 + lane * 16;
  bf16x8 k0 = lds_rd128(lb), k1 = lds_rd128(lb + 1024), v0 = lds_rd128(lb + 2048), v1;
  f32x16 sA = MFMA32(k0, q0, negm), sB;
  sA = MFMA32(k1, q1, sA);
  k0 = lds_rd128(lb + 4096); k1 = lds_rd128(lb + 4096 + 1024);
  u32x4 p0, p1 = (u32x4){0u, 0u, 0u, 0u};
  v1 = ones;
#define EXP4(S, i) do { S[i] = __builtin_amdgcn_exp2f(S[i]); S[i + 1] = __builtin_amdgcn_exp2f(S[i + 1]); \
    S[i + 2] = __builtin_amdgcn_exp2f(S[i + 2]); S[i + 3] = __builtin_amdgcn_exp2f(S[i + 3]); } while (0)
#define EXP2(S, i) do { S[i] = __builtin_amdgcn_exp2f(S[i]); S[i + 1] = __builtin_amdgcn_exp2f(S[i + 1]); } while (0)
#define PK4(P, S, i) do { P[0] = pk2(S[i], S[i + 1]); P[1] = pk2(S[i + 2], S[i + 3]); P[2] = pk2(S[i + 4], S[i + 5]); \
    P[3] = pk2(S[i + 6], S[i + 7]); } while (0)
#define BF(P) __builtin_bit_cast(bf16x8, P)
#define PIN(x) asm volatile("" : "+v"(x))
#define TILE(SC, SN, a0, a1, a2) do { \
    SN = MFMA32(k0, q0, negm); SBAR(); k0 = lds_rd128(a2); EXP4(SC, 0); PIN(SC); SBAR(); \
    o = MFMA32(v1, BF(p1), o); SBAR(); v1 = lds_rd128((a0) + 3072); EXP4(SC, 4); PIN(SC); SBAR(); \
    SN = MFMA32(k1, q1, SN); SBAR(); k1 = lds_rd128((a2) + 1024); PK4(p0, SC, 0); EXP2(SC, 8); PIN(SC); PIN(p0); SBAR(); \
    l = MFMA16(ones, BF(p1), l); SBAR(); EXP2(SC, 10); PIN(SC); SBAR(); \
    o = MFMA32(v0, BF(p0), o); SBAR(); v0 = lds_rd128((a1) + 2048); EXP4(SC, 12); PIN(SC); SBAR(); \
    l = MFMA16(ones, BF(p0), l); SBAR(); PK4(p1, SC, 8); PIN(p1); SBAR(); \
  } while (0)
  SBAR();
  for (int s = 0; s < 16; ++s) {
    const unsigned a = lb + (s & 3) * K2_STAGE, an = lb + ((s + 1) & 3) * K2_STAGE;
    TILE(sA, sB, a, a + 4096, a + 8192);
    TILE(sB, sA, a + 4096, a + 8192, a + 12288);
    TILE(sA, sB, a + 8192, a + 12288, an);
    TILE(sB, sA, a + 12288, an, an + 4096);
    if (s < 14) BARRIER_L();
    SBAR();
  }
  o = MFMA32(v1, BF(p1), o);
  l = MFMA16(ones, BF(p1), l);
  STR(1);
  STAMP(1, 2);
  float lq;
  {
    const float a = __shfl(l[0], lane & 15), b = __shfl(l[1], lane & 15);
    lq = (lane & 16) ? b : a;
  }
  if (__builtin_expect(!__all(lq >= 9.094947017729282e-13f && lq < 3.0e38f), 0)) {
    float mx = -3.0e38f;
    const f32x16 zero = (f32x16){};
    const char* kp = kvg + lane * 16;
    for (int kb = 0; kb < 64; ++kb) {
      f32x16 s = MFMA32(*(const bf16x8*)(kp + kb * 4096), q0, zero);
      s = MFMA32(*(const bf16x8*)(kp + kb * 4096 + 1024), q1, s);
#pragma unroll
      for (int r = 0; r < 16; ++r) mx = fmaxf(mx, s[r]);
    }
    mx = xhalf_max(mx);
#pragma unroll
    for (int r = 0; r < 16; ++r) negm[r] = -mx;
    o = (f32x16){};
    l = (f32x4){};
    for (int kb = 0; kb < 64; ++kb) {
      const char* a = kp + kb * 4096;
      attn_tile(*(const bf16x8*)a, *(const bf16x8*)(a + 1024), *(const bf16x8*)(a + 2048), *(const bf16x8*)(a + 3072),
                q0, q1, negm, ones, o, l);
    }
    const float a = __shfl(l[0], lane & 15), b = __shfl(l[1], lane & 15);
    lq = (lane & 16) ? b : a;
  }
  const __amdgpu_buffer_rsrc_t ors = __builtin_amdgcn_make_buffer_rsrc(ws + WS_O, 0, 4 << 20, 0x00020000);
  gu32* cnt = (gu32*)(ws + WS_CNT);
  {
    const float inv = 1.0f / lq;
    const unsigned oo = (tb * 64 + head * 2) * 1024 + vl;
    u32x4 oa = __builtin_bit_cast(u32x4, pack8(o, 0, inv)), ob2 = __builtin_bit_cast(u32x4, pack8(o, 1, inv));
    PIN(oa); PIN(ob2);
    __builtin_amdgcn_raw_buffer_store_b128(oa, ors, oo, 0, 16);
    __builtin_amdgcn_raw_buffer_store_b128(ob2, ors, oo + 1024, 0, 16);
    WAITV(0);
    STR(2);
    BARRIER_L();
    STR(3);
  }
  const int tw = wid & 3, kh = (wid >> 2) ^ kflip;
  if (wid == 0) {
    if (lane == 0) __hip_atomic_fetch_add(cnt + qb * 64, 1u, __ATOMIC_RELAXED, __HIP_MEMORY_SCOPE_AGENT);
#pragma unroll 1
    for (int it = 0; it < (1 << 22); ++it) {
      const unsigned v = __hip_atomic_load(cnt + (mt >> 1) * 64, __ATOMIC_RELAXED, __HIP_MEMORY_SCOPE_AGENT);
      if ((unsigned)__builtin_amdgcn_readfirstlane(v) >= 32u) break;
      __builtin_amdgcn_s_sleep(2);
    }
  }
  BARRIER();
  STR(5);
  const unsigned ob = (mt * 4 + tw) * 65536 + kh * 32768;
  constexpr int OD = OP_OD;
  bf16x8 of[OD];
  auto ldo = [&](int j) {
    of[j % OD] = __builtin_bit_cast(bf16x8, __builtin_amdgcn_raw_buffer_load_b128(ors, vl + (j & 3) * 1024, ob + (((j >> 2) + g0) & 7) * 4096, OP_AUX));
  };
#pragma unroll
  for (int j = 0; j < OD; ++j) { ldo(j); SBAR(); }
  const int hi = lane >> 5, q4 = (lane & 15) >> 2;
  const unsigned wl = (hi * 4 + q4) * 128 + ((q4 >> 1) * 64) + ((lane >> 4) & 1) * 32 + (lane & 3) * 8;
  const unsigned wbE = lds0 + K2_WE + kh * 16384, wbM = lds0 + K2_CV + kh * 32768, wbL = lds0 + kh * K2_STAGE;
  unsigned wa0 = wbE + wl, wa1 = wbE + (wl ^ 64);
  f32x16 acc0 = (f32x16){}, acc1 = (f32x16){};
  bf16x8 w0[3], w1[3];
  auto ldw = [&](int j) {
    if (j == 8) { wa0 += wbM - wbE; wa1 += wbM - wbE; }
    if (j == 24) { wa0 += wbL - wbM; wa1 += wbL - wbM; }
    const unsigned a = (j < 8 ? j : j < 24 ? j - 8 : j - 24) * 2048;
    w0[j % 3] = cat44(lds_tr(wa0 + a), lds_tr(wa0 + a + 1024));
    w1[j % 3] = cat44(lds_tr(wa1 + a), lds_tr(wa1 + a + 1024));
  };
  STR(6);
  ldw(0); ldw(1);
  SBAR();
#pragma unroll
  for (int j = 0; j < 32; ++j) {
    if (j == 1) STR(7);
    if (j + 2 < 32) ldw(j + 2);
    acc0 = MFMA32(w0[j % 3], of[j % OD], acc0);
    acc1 = MFMA32(w1[j % 3], of[j % OD], acc1);
    if (j + OD < 32) ldo(j + OD);
    SBAR();
  }
  STR(10);
  {
    const f32x16 snd = kh ? acc0 : acc1;
    const unsigned ra = lds0 + 32768 + wid * 4096 + vl;
#pragma unroll
    for (int i = 0; i < 4; ++i)
      *(LAS f32x4*)(uintptr_t)(ra + i * 1024) = (f32x4){snd[4 * i], snd[4 * i + 1], snd[4 * i + 2], snd[4 * i + 3]};
    BARRIER_L();
    STR(11);
    const f32x16 kp = kh ? acc1 : acc0;
    const unsigned rb = lds0 + 32768 + (wid ^ 4) * 4096 + vl;
    unsigned short* od = out + (size_t)((mt * 4 + tw) * 32 + (lane & 31)) * 2048 + 1024 + nt * 64 + kh * 32 + hi * 8;
    u32x2 a[4];
#pragma unroll
    for (int i = 0; i < 4; ++i) {
      const f32x4 r = *(const LAS f32x4*)(uintptr_t)(rb + i * 1024);
      a[i] = (u32x2){pk2(kp[4 * i] + r[0], kp[4 * i + 1] + r[1]), pk2(kp[4 * i + 2] + r[2], kp[4 * i + 3] + r[3])};
    }
#pragma unroll
    for (int i = 0; i < 2; ++i) {
      const auto x0 = __builtin_amdgcn_permlane32_swap(a[2 * i][0], a[2 * i + 1][0], false, false);
      const auto x1 = __builtin_amdgcn_permlane32_swap(a[2 * i][1], a[2 * i + 1][1], false, false);
      st16b(od + i * 16, (u32x4){x0[0], x1[0], x0[1], x1[1]});
    }
  }
  STR(12);
}

extern "C" void kernel_launch(void* const* d_in, const int* in_sizes, int n_in, void* d_out, int out_size,
                              void* d_ws, size_t ws_size, hipStream_t stream) {
  const unsigned short* aq = (const unsigned short*)d_in[0];
  const unsigned short* ak = (const unsigned short*)d_in[1];
  const unsigned short* av = (const unsigned short*)d_in[2];
  const unsigned short* shk = (const unsigned short*)d_in[3];
  const unsigned short* ssk = (const unsigned short*)d_in[4];
  const unsigned short* ssd = (const unsigned short*)d_in[5];
  const unsigned short* x = (const unsigned short*)d_in[6];
  const unsigned short* wqkv = (const unsigned short*)d_in[7];
  const unsigned short* wo = (const unsigned short*)d_in[8];
  char* ws = (char*)d_ws;
  unsigned short* out = (unsigned short*)d_out;
  (void)hipFuncSetAttribute((const void*)k1_qkv, hipFuncAttributeMaxDynamicSharedMemorySize, K1_LDS);
  (void)hipFuncSetAttribute((const void*)k2_attn_conv, hipFuncAttributeMaxDynamicSharedMemorySize, K2_LDS);
  k1_qkv<<<256, 768, K1_LDS, stream>>>(x, wqkv, ak, av, shk, ws);
  k2_attn_conv<<<256, 768, K2_LDS, stream>>>(aq, ssk, ssd, wo, ws, out);
}
```

```cpp
#include <hip/hip_runtime.h>
#include <hip/hip_bf16.h>
#include <stdint.h>

typedef short bf16x8 __attribute__((ext_vector_type(8)));
typedef short s16x4 __attribute__((ext_vector_type(4)));
typedef float f32x16 __attribute__((ext_vector_type(16)));
typedef float f32x4 __attribute__((ext_vector_type(4)));
typedef unsigned u32x4 __attribute__((ext_vector_type(4)));
typedef unsigned u32x2 __attribute__((ext_vector_type(2)));
typedef __bf16 bf16v2 __attribute__((ext_vector_type(2)));
typedef float f32x2 __attribute__((ext_vector_type(2)));
typedef unsigned u32x8 __attribute__((ext_vector_type(8)));
typedef __bf16 bf16v8 __attribute__((ext_vector_type(8)));
typedef __bf16 bf16v16 __attribute__((ext_vector_type(16)));
#define LAS __attribute__((address_space(3)))
#define DI __device__ __forceinline__
#define MFMA32(a, b, c) __builtin_amdgcn_mfma_f32_32x32x16_bf16((a), (b), (c), 0, 0, 0)
#define MFMA16(a, b, c) __builtin_amdgcn_mfma_f32_16x16x32_bf16((a), (b), (c), 0, 0, 0)

constexpr int S_ = 2048, D_ = 1024, NH = 32;
constexpr size_t WS_Q = 0;
constexpr size_t WS_KV = WS_Q + (4u << 20);
constexpr size_t WS_O = WS_KV + (8u << 20);
constexpr size_t WS_UR = WS_O + (4u << 20);
constexpr size_t WS_KMAX = WS_UR + (4u << 20);
constexpr size_t WS_CNT = WS_KMAX + 4096;
#define CONV_START 8

DI unsigned pk2(float lo, float hi) {
  f32x2 v = {lo, hi};
  bf16v2 b = __builtin_convertvector(v, bf16v2);
  return __builtin_bit_cast(unsigned, b);
}
DI float bflo(unsigned u) { return __uint_as_float(u << 16); }
DI float bfhi(unsigned u) { return __uint_as_float(u & 0xffff0000u); }
DI float sq2(unsigned u, float c) {
  const bf16v2 b = __builtin_bit_cast(bf16v2, u);
  return __builtin_amdgcn_fdot2_f32_bf16(b, b, c, false);
}
DI float rbf(float x) { return bflo(pk2(x, x)); }
DI bf16x8 pack8(const f32x16& x, int s, float scale) {
  u32x4 p;
  p[0] = pk2(x[8 * s + 0] * scale, x[8 * s + 1] * scale);
  p[1] = pk2(x[8 * s + 2] * scale, x[8 * s + 3] * scale);
  p[2] = pk2(x[8 * s + 4] * scale, x[8 * s + 5] * scale);
  p[3] = pk2(x[8 * s + 6] * scale, x[8 * s + 7] * scale);
  return __builtin_bit_cast(bf16x8, p);
}
DI bf16x8 pack8n(const f32x16& x, int s) {
  u32x4 p;
  p[0] = pk2(x[8 * s + 0], x[8 * s + 1]);
  p[1] = pk2(x[8 * s + 2], x[8 * s + 3]);
  p[2] = pk2(x[8 * s + 4], x[8 * s + 5]);
  p[3] = pk2(x[8 * s + 6], x[8 * s + 7]);
  return __builtin_bit_cast(bf16x8, p);
}
DI void glds16(const void* gbase, unsigned voff, unsigned lds_dst) {
  unsigned keep;
  asm volatile("s_mov_b32 %0, m0\n\ts_mov_b32 m0, %3\n\ts_nop 0\n\tglobal_load_lds_dwordx4 %1, %2\n\ts_mov_b32 m0, %0"
               : "=&s"(keep) : "v"(voff), "s"(gbase), "s"(lds_dst) : "memory");
}
DI bf16x8 lds_rd128(unsigned a) { return *(const LAS bf16x8*)(uintptr_t)a; }
DI s16x4 lds_tr(unsigned a) {
  typedef short v4i16_t __attribute__((ext_vector_type(4)));
  return __builtin_amdgcn_ds_read_tr16_b64_v4i16((LAS v4i16_t*)(uintptr_t)a);
}
DI bf16x8 cat44(s16x4 lo, s16x4 hi) { return __builtin_shufflevector(lo, hi, 0, 1, 2, 3, 4, 5, 6, 7); }
#define SBAR() __builtin_amdgcn_sched_barrier(0)
#define SGB(m, n) __builtin_amdgcn_sched_group_barrier((m), (n), 0)
#define WAITV(n) asm volatile("s_waitcnt vmcnt(" #n ")" ::: "memory")
#define BARRIER_L() asm volatile("s_waitcnt lgkmcnt(0)\n\ts_barrier" ::: "memory")
#define BARRIER() asm volatile("s_barrier" ::: "memory")
#define TB_DECL do { } while (0)
#define TPRE(i) do { } while (0)
#define TB_OUT() do { } while (0)
#define KB(i) BARRIER()
#define KBL(i) BARRIER_L()
#define STR1(i) do { } while (0)
#define STR(i) do { } while (0)
#define STAMP(k, i) do { } while (0)
#define STAMPS(k, i) do { } while (0)
DI float xhalf_sum(float v) {
  auto r = __builtin_amdgcn_permlane32_swap(__float_as_uint(v), __float_as_uint(v), false, false);
  return __uint_as_float(r[0]) + __uint_as_float(r[1]);
}
DI float xhalf_max(float v) {
  auto r = __builtin_amdgcn_permlane32_swap(__float_as_uint(v), __float_as_uint(v), false, false);
  return fmaxf(__uint_as_float(r[0]), __uint_as_float(r[1]));
}

constexpr int K1_STAGE = 40960, K1_LDS = 4 * K1_STAGE;
constexpr float QSCALE = 0.17677669529663687f * 1.4426950408889634f;
#define K1_MAP 0
#define K1_STAG 2
#define K1_PF 1
#define K1_PRIO 0
#define K1_ST 2
DI void st16(void* p, u32x4 v) {
  asm volatile("global_store_dwordx4 %0, %1, off sc1\n\ts_nop 1" :: "v"(p), "v"(v) : "memory");
}

__global__ __launch_bounds__(768, 3) void k1_qkv(const unsigned short* x, const unsigned short* w,
                                                 const unsigned short* ak, const unsigned short* av,
                                                 const unsigned short* shk, char* ws) {
  extern __shared__ __attribute__((aligned(16))) char lds[];
  const unsigned lds0 = (unsigned)(uintptr_t)(LAS char*)lds;
  const int tid = threadIdx.x, lane = tid & 63;
  const int wid = __builtin_amdgcn_readfirstlane(tid >> 6);
  const int bx = blockIdx.x, xg = bx & 7, li = bx >> 3;
  const int mt = 8 * (xg & 1) + (li & 7), nt = 4 * (xg >> 1) + (li >> 3);
  STAMP(0, 0);
  STR1(0);
#define K1_S0 ((li & 7) + (li >> 3))
  const int s0 = K1_S0;
  if (wid >= 8) {
    const int cg = lane & 7, pr = (lane >> 4) & 1, pg = (lane >> 5) * 2 + ((lane >> 3) & 1);
    const int tb = (bx >> 4) * 4 + (wid - 8);
    const int ch0 = (bx & 15) * 64 + cg * 8, t0 = tb * 32 + pg * 8 + pr * 4;
    u32x4 kr[7], vr[4], wk[4];
    char* ur = ws + WS_UR + ((bx & 15) * 64 + tb) * 4096 + cg * 128 + pr * 64 + (3 - pg) * 16;
    if (bx == 0 && tid - 512 < 8) ((unsigned*)(ws + WS_CNT))[(tid - 512) * 64] = 0u;
    auto ldk = [&](int r) { const int t = t0 - 3 + r; kr[r] = *(const u32x4*)(ak + (size_t)(t < 0 ? 0 : t) * 1024 + ch0); };
    auto ldv = [&](int r) { vr[r] = *(const u32x4*)(av + (size_t)(t0 + r) * 1024 + ch0); };
    auto ldw = [&](int r) { wk[r] = *(const u32x4*)(shk + (size_t)ch0 * 4 + r * 8); };
#define UR_ST 2
#define PL0 11
#define PLN 6
#define PC0 15
#define PCN 4
    auto ld = [&](int i) { if (i < 7) ldk(i); else if (i < 11) ldv(i - 7); };
    ldw(0); ldw(1); ldw(2); ldw(3);
    auto cmp = [&](int j) {
      const f32x2 w0 = {bflo(wk[j][0]), bflo(wk[j][2])}, w1 = {bfhi(wk[j][0]), bfhi(wk[j][2])};
      const f32x2 w2 = {bflo(wk[j][1]), bflo(wk[j][3])}, w3 = {bfhi(wk[j][1]), bfhi(wk[j][3])};
      f32x2 kf[7], u[4];
#pragma unroll
      for (int r = 0; r < 7; ++r) kf[r] = (f32x2){bflo(kr[r][j]), bfhi(kr[r][j])};
#pragma unroll
      for (int r = 0; r < 4; ++r) {
        const f32x2 a = __builtin_elementwise_fma(kf[r + 3], w0, __builtin_elementwise_fma(kf[r + 2], w1,
                        __builtin_elementwise_fma(kf[r + 1], w2, kf[r] * w3)));
        u[r] = a * (f32x2){bflo(vr[r][j]), bfhi(vr[r][j])};
      }
      const auto s0 = __builtin_amdgcn_permlane16_swap(pk2(u[3][0], u[2][0]), pk2(u[3][1], u[2][1]), false, false);
      const auto s1 = __builtin_amdgcn_permlane16_swap(pk2(u[1][0], u[0][0]), pk2(u[1][1], u[0][1]), false, false);
      st16(ur + j * 1024, (u32x4){s0[1], s1[1], s0[0], s1[0]});
    };
#pragma unroll
    for (int b = 0; b < 16; ++b) {
      SBAR(); BARRIER(); SBAR();
#pragma unroll
      for (int i = 0; i < PLN; ++i) if (b >= PL0) ld((b - PL0) * PLN + i);
      if (b == PC0 && t0 == 0) { kr[0] = (u32x4){0u, 0u, 0u, 0u}; kr[1] = kr[0]; kr[2] = kr[0]; }
#pragma unroll
      for (int c = 0; c < 4; ++c) if (b >= PC0 && (c / PCN == b - PC0 || (b == 15 && c / PCN > 15 - PC0))) cmp(c);
      if (b >= PC0 && (4 + PCN - 1) / PCN - 1 <= b - PC0) break;
    }
    return;
  }
  if (wid >= 4) {
    const int lw = wid - 4;
    const unsigned rowl = lane >> 3, sl = lane & 7;
    unsigned vx[4], vw[6];
#pragma unroll
    for (int i = 0; i < 4; ++i)
      vx[i] = (i * 8 + rowl) * 2048 + ((sl ^ (((i & 1) * 4 + (lane >> 4)) & 7)) * 16) - i * 1024;
#pragma unroll
    for (int i = 0; i < 6; ++i) {
      const int q = lw * 6 + i, sec = q >> 3, p = q & 7;
      vw[i] = (p * 8 + rowl) * 6144 + sec * 2048 + ((sl ^ (((lane >> 4) & 1) << 1) ^ ((i & 1) << 2)) * 16) - (i % 3) * 1024;
    }
    const char* xb = (const char*)(x + (size_t)(mt * 128 + lw * 32) * 1024);
    const char* wb = (const char*)(w + nt * 64);
    auto issue = [&](int s, int buf) {
#define K1_WIN 4
      const int ks = (s & (16 - K1_WIN)) | ((s + s0) & (K1_WIN - 1));
      const unsigned dst = lds0 + buf * K1_STAGE;
      const char* xs = xb + ks * 128;
      const char* wsrc = wb + (size_t)ks * 64 * 6144;
      asm volatile("s_mov_b32 m0, %0\n\ts_nop 0\n\t"
                   "global_load_lds_dwordx4 %3, %1\n\tglobal_load_lds_dwordx4 %4, %1 offset:1024\n\t"
                   "global_load_lds_dwordx4 %5, %1 offset:2048\n\tglobal_load_lds_dwordx4 %6, %1 offset:3072\n\t"
                   "s_mov_b32 m0, %13\n\ts_nop 0\n\t"
                   "global_load_lds_dwordx4 %7, %2\n\tglobal_load_lds_dwordx4 %8, %2 offset:1024\n\t"
                   "global_load_lds_dwordx4 %9, %2 offset:2048\n\t"
                   "s_mov_b32 m0, %14\n\ts_nop 0\n\t"
                   "global_load_lds_dwordx4 %10, %2\n\tglobal_load_lds_dwordx4 %11, %2 offset:1024\n\t"
                   "global_load_lds_dwordx4 %12, %2 offset:2048"
                   :: "s"(dst + lw * 4096), "s"(xs), "s"(wsrc), "v"(vx[0]), "v"(vx[1]), "v"(vx[2]), "v"(vx[3]),
                      "v"(vw[0]), "v"(vw[1]), "v"(vw[2]), "v"(vw[3]), "v"(vw[4]), "v"(vw[5]),
                      "s"(dst + 16384 + lw * 6144), "s"(dst + 16384 + lw * 6144 + 3072) : "memory");
    };
    issue(0, 0); issue(1, 1);
    WAITV(10); BARRIER();
    issue(2, 2); issue(3, 3);
    for (int s = 0; s < 12; ++s) {
      WAITV(20); BARRIER();
      issue(s + 4, s & 3);
    }
    WAITV(20); BARRIER();
    WAITV(10); BARRIER();
    WAITV(0); BARRIER();
    return;
  }
  const int wm = wid & 1, wn = wid >> 1;
  const int c16 = lane & 15, g = lane >> 4, q4 = c16 >> 2;
  const unsigned xa = lds0 + (wm * 64 + c16) * 128 + ((g ^ (c16 >> 1)) * 16);
  const unsigned wa = lds0 + 16384 + (8 * g + q4) * 128 + (((2 * wn) ^ ((q4 >> 1) | ((g & 1) << 1))) * 32) + (lane & 3) * 8;
  f32x4 acc[4][6];
#pragma unroll
  for (int i = 0; i < 4; ++i)
#pragma unroll
    for (int j = 0; j < 6; ++j) acc[i][j] = (f32x4){};
  bf16x8 xf[2][4], wf[6];
  unsigned xa0 = xa, xa1 = xa ^ 64, wa0 = wa, wa1 = wa ^ 32;
  auto ldx = [&](int kk, int i) { xf[kk][i] = lds_rd128((kk ? xa1 : xa0) + i * 2048); };
  auto ldw = [&](int kk, int j) {
    const unsigned a = ((j & 1) ? wa1 : wa0) + (j >> 1) * 8192 + kk * 4096;
    wf[j] = cat44(lds_tr(a), lds_tr(a + 512));
  };
  auto mm = [&](int kk, int j) {
#pragma unroll
    for (int i = 0; i < 4; ++i) {
      if (j < 4) acc[i][j] = MFMA16(wf[j], xf[kk][i], acc[i][j]);
      else acc[i][j] = MFMA16(xf[kk][i], wf[j], acc[i][j]);
    }
  };
#define ILV4() do { SGB(0x8, 1); SGB(0x100, 1); SGB(0x8, 1); SGB(0x100, 1); SGB(0x8, 1); SGB(0x100, 1); \
    SGB(0x8, 1); SGB(0x100, 1); SBAR(); } while (0)
  BARRIER();
#pragma unroll
  for (int j = 0; j < 5; ++j) ldw(0, j);
#pragma unroll
  for (int i = 0; i < 4; ++i) ldx(0, i);
  unsigned so = 0;
  SBAR();
#define K1_HEAD() do { mm(0, 0); ldw(0, 5); ILV4(); \
    mm(0, 1); ldw(1, 0); ldx(1, 0); ILV4(); \
    mm(0, 2); ldw(1, 1); ldx(1, 1); ILV4(); \
    mm(0, 3); ldw(1, 2); ldx(1, 2); ILV4(); \
    mm(0, 4); ldw(1, 3); ldx(1, 3); ILV4(); \
    mm(0, 5); ldw(1, 4); ILV4(); \
    mm(1, 0); ldw(1, 5); ILV4(); \
    mm(1, 1); SBAR(); } while (0)
  K1_HEAD();
  for (int s = 0; s < 15; ++s) {
    BARRIER_L();
    {
      const unsigned d = (so == 3 * K1_STAGE) ? (unsigned)(-3 * K1_STAGE) : (unsigned)K1_STAGE;
      so += d; xa0 += d; xa1 += d; wa0 += d; wa1 += d;
    }
    SBAR();
    mm(1, 2); ldw(0, 0); ldx(0, 0); ldx(0, 1); ILV4();
    mm(1, 3); ldw(0, 1); ldx(0, 2); ldx(0, 3); ILV4();
    mm(1, 4); ldw(0, 2); ldw(0, 3); ILV4();
    mm(1, 5); ldw(0, 4); ILV4();
    K1_HEAD();
  }
  mm(1, 2); mm(1, 3); mm(1, 4); mm(1, 5);
  const int head = nt * 2 + wn;
  float kn = 0.f;
  const int fo = (g >> 1) * 1024 + (g & 1) * 512;
#pragma unroll
  for (int i = 0; i < 4; ++i) {
    const int tb = mt * 4 + wm * 2 + (i >> 1), u = i & 1;
    char* qd = ws + WS_Q + ((size_t)(head * 64 + tb) * 2) * 1024 + fo;
    char* kvd = ws + WS_KV + ((size_t)(head * 64 + tb) * 4) * 1024 + fo;
    const u32x4 qp = {pk2(acc[i][0][0] * QSCALE, acc[i][0][1] * QSCALE), pk2(acc[i][0][2] * QSCALE, acc[i][0][3] * QSCALE),
                      pk2(acc[i][1][0] * QSCALE, acc[i][1][1] * QSCALE), pk2(acc[i][1][2] * QSCALE, acc[i][1][3] * QSCALE)};
    st16(qd + (16 * u + c16) * 16, qp);
    const u32x4 kp = {pk2(acc[i][2][0], acc[i][2][1]), pk2(acc[i][2][2], acc[i][2][3]),
                      pk2(acc[i][3][0], acc[i][3][1]), pk2(acc[i][3][2], acc[i][3][3])};
    st16(kvd + ((c16 & 7) + 8 * u + 16 * (c16 >> 3)) * 16, kp);
    float ss = 0.f;
#pragma unroll
    for (int e = 0; e < 4; ++e) ss = sq2(kp[e], ss);
    ss += __shfl_xor(ss, 16);
    kn = fmaxf(kn, xhalf_sum(ss));
    const f32x4 va = acc[i & 2][4 + u], vb = acc[(i & 2) + 1][4 + u];
    const u32x4 vp = {pk2(va[0], va[1]), pk2(va[2], va[3]), pk2(vb[0], vb[1]), pk2(vb[2], vb[3])};
    st16(kvd + 2048 + (16 * u + c16) * 16, vp);
  }
#pragma unroll
  for (int o = 1; o < 16; o <<= 1) kn = fmaxf(kn, __shfl_xor(kn, o));
  if (lane == 0) ((float*)(ws + WS_KMAX))[head * 32 + mt * 2 + wm] = kn;
}

constexpr int K2_STAGE = 16384;
constexpr int K2_WE = 65536;
constexpr int K2_CV = 98304, CV_SZ = 16384;
constexpr int CV_URE = 0, CV_URO = 4160, CV_HP = 8320, CV_Z = 0;
constexpr int K2_LDS = 163840;
typedef __attribute__((address_space(1))) unsigned gu32;
#define ATT_PRIO 0

DI void glds4(const void* gbase, unsigned v0, unsigned v1, unsigned v2, unsigned v3, unsigned lds_dst) {
  asm volatile("s_mov_b32 m0, %5\n\ts_nop 0\n\t"
               "global_load_lds_dwordx4 %1, %0\n\tglobal_load_lds_dwordx4 %2, %0 offset:1024\n\t"
               "global_load_lds_dwordx4 %3, %0 offset:2048\n\tglobal_load_lds_dwordx4 %4, %0 offset:3072"
               :: "s"(gbase), "v"(v0), "v"(v1), "v"(v2), "v"(v3), "s"(lds_dst) : "memory");
}

#define STA 0
#define STB 0
DI void st8a(void* p, u32x2 v) {
  *(u32x2*)p = v;
}
DI void st16b(void* p, u32x4 v) {
  *(u32x4*)p = v;
}

#define APT 0x2110
#define APRI(t) do { __builtin_amdgcn_s_setprio((APT >> (12 - 4 * (t))) & 3); SBAR(); } while (0)
#define HP0 3
#define HPC 0
#define CPACE() do { } while (0)
DI void attn_tile(const bf16x8 k0, const bf16x8 k1, const bf16x8 v0, const bf16x8 v1, const bf16x8 q0, const bf16x8 q1,
                  const f32x16& negm, const bf16x8 ones, f32x16& o, f32x4& l) {
  f32x16 s = MFMA32(k0, q0, negm);
  s = MFMA32(k1, q1, s);
#pragma unroll
  for (int r = 0; r < 16; ++r) s[r] = __builtin_amdgcn_exp2f(s[r]);
  const bf16x8 p0 = pack8n(s, 0), p1 = pack8n(s, 1);
  o = MFMA32(v0, p0, o);
  o = MFMA32(v1, p1, o);
  l = MFMA16(ones, p0, l);
  l = MFMA16(ones, p1, l);
}

__global__ __launch_bounds__(768, 3) void k2_attn_conv(const unsigned short* aq, const unsigned short* ssk,
                                                       const unsigned short* ssd, const unsigned short* wo,
                                                       char* ws, unsigned short* out) {
  extern __shared__ __attribute__((aligned(16))) char lds[];
  const unsigned lds0 = (unsigned)(uintptr_t)(LAS char*)lds;
  const int tid = threadIdx.x, lane = tid & 63;
  const int wid = __builtin_amdgcn_readfirstlane(tid >> 6);
  const int bx = blockIdx.x, xg = bx & 7, li = bx >> 3;
  const int head = xg * 4 + (li >> 3), qb = li & 7;
  const int mt = 4 * (xg & 3) + (li & 3), nt = 8 * (xg >> 2) + (li >> 2);
  const char* kvg = ws + WS_KV + (size_t)head * 262144;
  const unsigned vl = lane * 16;
  const unsigned vwo = (lane >> 3) * 2048 + (((lane & 7) ^ (((lane >> 4) & 1) << 2)) * 16);
  const char* wob = (const char*)(wo + nt * 64);
#define OP_STAG 0
#define OP_AUX 16
#define OP_OD 20
  const int g0 = (OP_STAG & 1) ? (li >> 3) * 2 : 0, kflip = (OP_STAG & 2) ? (li >> 2) & 1 : 0;
  auto wo4 = [&](int half, int pos, unsigned dst) {
    const int row0 = half * 512 + ((pos + 4 * g0) & 31) * 16;
    glds4(wob + (size_t)row0 * 2048, vwo, vwo + 15360, vwo + 30720, vwo + 46080, dst);
  };
  STAMP(1, 0);
  STR(0);
  TB_DECL;
  TPRE(0);
  if (wid >= 8) {
    __builtin_amdgcn_s_setprio(HP0);
    const int cw = wid - 8;
    const int chg = xg * 32 + li;
    const int ch = chg * 4 + cw;
    const unsigned cb = lds0 + K2_CV + cw * CV_SZ;
    auto issue = [&](int s) { glds4(kvg + (size_t)s * K2_STAGE + cw * 4096, vl, vl, vl, vl, lds0 + (s & 3) * K2_STAGE + cw * 4096); };
    u32x2 qv[8];
    auto ldq = [&](int i) {
      qv[i] = *(const u32x2*)(aq + (size_t)(((i >> 2) * 256 + cw * 64 + lane) * 4 + (i & 3)) * 1024 + chg * 4);
    };
    {
      const unsigned vu = (64 - (lane >> 2)) * 4096 + (lane & 3) * 16;
      glds4(ws + WS_UR - 4096 + (ch >> 6) * 262144 + ((ch >> 1) & 3) * 1024 + ((ch >> 3) & 7) * 128 + (ch & 1) * 64,
            vu, vu - 66560, vu - 2 * 66560, vu - 3 * 66560, cb + CV_URE);
    }
    const unsigned dd = *(const __attribute__((address_space(4))) unsigned*)(uintptr_t)(ssd + (ch & ~1));
    if (lane < 30) *(LAS u32x4*)(uintptr_t)(cb + CV_HP + lane * 16) = (u32x4){0u, 0u, 0u, 0u};
    if (lane < 4) *(LAS u32x4*)(uintptr_t)(cb + CV_URE + 4096 + lane * 16) = (u32x4){0u, 0u, 0u, 0u};
    if (lane < 4) *(LAS u32x4*)(uintptr_t)(cb + CV_URO + 4096 + lane * 16) = (u32x4){0u, 0u, 0u, 0u};
    KB(0);
    issue(2);
    glds4(ssk + (size_t)ch * 2048, vl, vl, vl, vl, cb + CV_HP + 480);
    issue(3);
    ldq(0);
    WAITV(13);
#pragma unroll
    for (int i = 0; i < 4; ++i) {
      const unsigned a = cb + CV_URE + i * 1024 + lane * 16;
      const u32x4 d = *(const LAS u32x4*)(uintptr_t)a;
      const unsigned nx = *(const LAS unsigned*)(uintptr_t)(a + 16);
      u32x4 o;
      o[0] = __builtin_amdgcn_alignbit(d[1], d[0], 16);
      o[1] = __builtin_amdgcn_alignbit(d[2], d[1], 16);
      o[2] = __builtin_amdgcn_alignbit(d[3], d[2], 16);
      o[3] = __builtin_amdgcn_alignbit(nx, d[3], 16);
      *(LAS u32x4*)(uintptr_t)(cb + CV_URO + i * 1024 + lane * 16) = o;
    }
    const int i16 = lane & 15, g = lane >> 4;
    f32x4 y[8];
    {
      const float dv = (ch & 1) ? bfhi(dd) : bflo(dd);
#pragma unroll
      for (int n = 0; n < 8; ++n) {
        const u32x2 uu = *(const LAS u32x2*)(uintptr_t)(cb + CV_URE + (2044 - (256 * n + 16 * i16 + 4 * g)) * 2);
        y[n] = (f32x4){bfhi(uu[1]) * dv, bflo(uu[1]) * dv, bfhi(uu[0]) * dv, bflo(uu[0]) * dv};
        asm volatile("" : "+v"(y[n]));
      }
    }
    unsigned ua = cb + ((i16 & 1) ? CV_URE : CV_URO) + (903 - (i16 >> 1) + 4 * g) * 4 - 7 * 512;
    unsigned ha = cb + CV_HP + i16 * 32 + g * 16;
    bf16x8 uf[8], hf[8];
    auto ldu = [&](int d) {
      u32x4 t;
#pragma unroll
      for (int j = 0; j < 4; ++j) t[j] = *(const LAS unsigned*)(uintptr_t)(ua + (7 - d) * 512 + j * 4);
      uf[d] = __builtin_bit_cast(bf16x8, t);
    };
    auto ldh = [&](int d) { hf[d] = lds_rd128(ha + d * 512); };
#pragma unroll
    for (int d = 0; d < 8; ++d) ldu(d);
    SBAR();
    STR(1);
#pragma unroll
    for (int rho = 0; rho < 8; ++rho) {
      TPRE(rho + 1);
      if (rho == 0) WAITV(5); else if (rho == 2 || rho == 4) WAITV(10); else if (rho == 3) WAITV(14); else WAITV(6);
      KB(rho + 1); issue(rho + 4);
      if (rho < 7) ldq(rho + 1);
      SBAR();
      if (rho == 1 || rho == 2) wo4(rho - 1, cw * 2, lds0 + K2_WE + (rho - 1) * 16384 + cw * 4096);
      if (rho == 0) {
#pragma unroll
        for (int d = 0; d < 8; ++d) ldh(7 - d);
      }
      ua += 64; ha += 64;
      if (HPC != HP0) __builtin_amdgcn_s_setprio(HPC);
      SBAR();
#pragma unroll
      for (int p = 7; p >= 1; --p) {
#pragma unroll
        for (int n = p; n < 8; ++n) { y[n] = MFMA16(uf[n - p], hf[p], y[n]); CPACE(); }
        SBAR();
        if (rho < 7) ldh(p);
        SBAR();
      }
#pragma unroll
      for (int n = 0; n < 8; ++n) {
        y[n] = MFMA16(uf[n], hf[0], y[n]);
        SBAR();
        if (rho < 7) ldu(n);
        SBAR();
        CPACE();
      }
      if (rho < 7) ldh(0);
      if (HPC != HP0) __builtin_amdgcn_s_setprio(HP0);
      SBAR();
    }
    STR(2);
#pragma unroll
    for (int n = 0; n < 8; ++n) {
      const u32x2 o = {pk2(y[n][0], y[n][1]), pk2(y[n][2], y[n][3])};
      *(LAS u32x2*)(uintptr_t)(cb + CV_Z + (256 * n + 16 * i16 + 4 * g) * 2) = o;
    }
    SBAR();
    TPRE(9);
#pragma unroll
    for (int i = 0; i < 8; ++i) asm volatile("" : "+v"(qv[i]));
    WAITV(0); KBL(9); issue(12);
    STR(3);
    u32x2 zz[2][4];
#pragma unroll
    for (int h = 0; h < 2; ++h)
#pragma unroll
      for (int c = 0; c < 4; ++c)
        zz[h][c] = *(const LAS u32x2*)(uintptr_t)(lds0 + K2_CV + c * CV_SZ + CV_Z + (h * 256 + cw * 64 + lane) * 8);
    auto sto = [&](int i) {
      const int h = i >> 2, r = i & 3;
      float z[4];
#pragma unroll
      for (int c = 0; c < 4; ++c) z[c] = (r & 1) ? bfhi(zz[h][c][r >> 1]) : bflo(zz[h][c][r >> 1]);
      const u32x2 q = qv[i];
      const u32x2 o = {pk2(bflo(q[0]) * z[0], bfhi(q[0]) * z[1]), pk2(bflo(q[1]) * z[2], bfhi(q[1]) * z[3])};
      st8a(out + (size_t)((h * 256 + cw * 64 + lane) * 4 + r) * 2048 + chg * 4, o);
    };
    SBAR();
    sto(0); sto(1);
    SBAR();
    STR(4);
    TPRE(10); KBL(10); issue(13);
    wo4(cw >> 1, 8 + (cw & 1) * 8, cb); wo4(cw >> 1, 10 + (cw & 1) * 8, cb + 4096);
    SBAR(); sto(2); sto(3); SBAR();
    TPRE(11); WAITV(16); KB(11); issue(14);
    wo4(cw >> 1, 12 + (cw & 1) * 8, cb + 8192); wo4(cw >> 1, 14 + (cw & 1) * 8, cb + 12288);
    SBAR(); sto(4); sto(5); SBAR();
    TPRE(12); WAITV(24); KB(12); issue(15);
    SBAR(); sto(6); sto(7); SBAR();
    TPRE(13); WAITV(16); KB(13);
    wo4(0, 24 + cw * 2, lds0 + cw * 4096);
    TPRE(14); WAITV(6); KB(14);
    wo4(1, 24 + cw * 2, lds0 + K2_STAGE + cw * 4096);
    WAITV(0);
    STR(5);
    KB(15);
    TB_OUT();
    return;
  }
  const int tb = qb * 8 + wid;
  const char* qg = ws + WS_Q + ((size_t)(head * 64 + tb) * 2) * 1024 + lane * 16;
  glds4(kvg + wid * 4096, vl, vl, vl, vl, lds0 + wid * 4096);
  const bf16x8 q0 = *(const bf16x8*)qg, q1 = *(const bf16x8*)(qg + 1024);
  unsigned kmu = 0u;
  {
    const __attribute__((address_space(4))) unsigned* kp = (const __attribute__((address_space(4))) unsigned*)(uintptr_t)(ws + WS_KMAX) + head * 32;
#pragma unroll
    for (int i = 0; i < 32; ++i) kmu = max(kmu, kp[i]);
  }
  float qs = 0.f;
  {
    const u32x4 a = __builtin_bit_cast(u32x4, q0), b = __builtin_bit_cast(u32x4, q1);
#pragma unroll
    for (int e = 0; e < 4; ++e) {
      qs = sq2(a[e], qs);
      qs = sq2(b[e], qs);
    }
    qs = xhalf_sum(qs);
  }
  const float mb = __builtin_amdgcn_sqrtf(qs * __uint_as_float(kmu)) * 1.0009765625f;
  f32x16 negm;
#pragma unroll
  for (int r = 0; r < 16; ++r) negm[r] = -mb;
  asm volatile("" : "+v"(negm));
  bf16x8 ones;
  {
    const int r = lane & 15;
#pragma unroll
    for (int e = 0; e < 8; ++e) ones[e] = (r < 4 && (r >> 1) == (e >> 2)) ? (short)0x3f80 : (short)0;
  }
  int sidx = (lane & 1) ? 0xEEEE : 0x4444;
#define LSUM(P, l) __builtin_amdgcn_smfmac_f32_16x16x64_bf16(__builtin_bit_cast(bf16v8, ones), __builtin_bit_cast(bf16v16, P), l, sidx, 0, 0)
  f32x16 o = (f32x16){};
  f32x4 l = (f32x4){};
  u32x8 P = (u32x8){};
  bf16x8 v1 = ones;
  asm volatile("" : "+v"(o), "+v"(l), "+v"(P), "+v"(v1), "+v"(ones), "+v"(sidx));
  WAITV(0);
  KB(0);
  STAMP(1, 1);
  const unsigned lb = lds0 + lane * 16;
  bf16x8 k0 = lds_rd128(lb), k1 = lds_rd128(lb + 1024), v0 = lds_rd128(lb + 2048);
  f32x16 sA = MFMA32(k0, q0, negm), sB;
  sA = MFMA32(k1, q1, sA);
  k0 = lds_rd128(lb + 4096); k1 = lds_rd128(lb + 4096 + 1024);
  u32x4 p0, p1 = (u32x4){0u, 0u, 0u, 0u};
#define EXP4(S, i) do { S[i] = __builtin_amdgcn_exp2f(S[i]); S[i + 1] = __builtin_amdgcn_exp2f(S[i + 1]); \
    S[i + 2] = __builtin_amdgcn_exp2f(S[i + 2]); S[i + 3] = __builtin_amdgcn_exp2f(S[i + 3]); } while (0)
#define EXP2(S, i) do { S[i] = __builtin_amdgcn_exp2f(S[i]); S[i + 1] = __builtin_amdgcn_exp2f(S[i + 1]); } while (0)
#define PK4(P, S, i) do { P[0] = pk2(S[i], S[i + 1]); P[1] = pk2(S[i + 2], S[i + 3]); P[2] = pk2(S[i + 4], S[i + 5]); \
    P[3] = pk2(S[i + 6], S[i + 7]); } while (0)
#define BF(P) __builtin_bit_cast(bf16x8, P)
#define PIN(x) asm volatile("" : "+v"(x))
#define PK4H(h, S, i) do { P[4 * h] = pk2(S[i], S[i + 1]); P[4 * h + 1] = pk2(S[i + 2], S[i + 3]); \
    P[4 * h + 2] = pk2(S[i + 4], S[i + 5]); P[4 * h + 3] = pk2(S[i + 6], S[i + 7]); } while (0)
#define PLO BF(__builtin_shufflevector(P, P, 0, 1, 2, 3))
#define PHI BF(__builtin_shufflevector(P, P, 4, 5, 6, 7))
#define TV 0
#define PK2(j, S) do { P[j] = pk2(S[2 * (j)], S[2 * (j) + 1]); P[(j) + 1] = pk2(S[2 * (j) + 2], S[2 * (j) + 3]); } while (0)
#define TILE(SC, SN, a0, a1, a2) do { \
    SN = MFMA32(k0, q0, negm); SBAR(); k0 = lds_rd128(a2); EXP4(SC, 0); PIN(SC); SBAR(); \
    o = MFMA32(v1, PHI, o); SBAR(); v1 = lds_rd128((a0) + 3072); EXP4(SC, 4); PIN(SC); SBAR(); \
    l = LSUM(P, l); SBAR(); EXP2(SC, 8); PIN(SC); SBAR(); \
    SN = MFMA32(k1, q1, SN); SBAR(); k1 = lds_rd128((a2) + 1024); PK4H(0, SC, 0); EXP2(SC, 10); PIN(SC); PIN(P); SBAR(); \
    o = MFMA32(v0, PLO, o); SBAR(); v0 = lds_rd128((a1) + 2048); EXP4(SC, 12); PIN(SC); SBAR(); \
    PK4H(1, SC, 8); PIN(P); SBAR(); \
  } while (0)
  SBAR();
  for (int s = 0; s < 16; ++s) {
    const unsigned a = lb + (s & 3) * K2_STAGE, an = lb + ((s + 1) & 3) * K2_STAGE;
    APRI(0); TILE(sA, sB, a, a + 4096, a + 8192);
    APRI(1); TILE(sB, sA, a + 4096, a + 8192, a + 12288);
    APRI(2); TILE(sA, sB, a + 8192, a + 12288, an);
    APRI(3); TILE(sB, sA, a + 12288, an, an + 4096);
    if (s < 14) KBL(s + 1);
    SBAR();
  }
  o = MFMA32(v1, PHI, o);
  l = LSUM(P, l);
#define LQ() do { const float a = __shfl(l[0] + l[1], lane & 15), b = __shfl(l[2] + l[3], lane & 15); lq = (lane & 16) ? b : a; } while (0)
  STR(1);
  TPRE(15);
  STAMP(1, 2);
  float lq;
  LQ();
  if (__builtin_expect(!__all(lq >= 9.094947017729282e-13f && lq < 3.0e38f), 0)) {
    float mx = -3.0e38f;
    const f32x16 zero = (f32x16){};
    const char* kp = kvg + lane * 16;
    for (int kb = 0; kb < 64; ++kb) {
      f32x16 s = MFMA32(*(const bf16x8*)(kp + kb * 4096), q0, zero);
      s = MFMA32(*(const bf16x8*)(kp + kb * 4096 + 1024), q1, s);
#pragma unroll
      for (int r = 0; r < 16; ++r) mx = fmaxf(mx, s[r]);
    }
    mx = xhalf_max(mx);
#pragma unroll
    for (int r = 0; r < 16; ++r) negm[r] = -mx;
    o = (f32x16){};
    l = (f32x4){};
    for (int kb = 0; kb < 64; ++kb) {
      const char* a = kp + kb * 4096;
      f32x16 s = MFMA32(*(const bf16x8*)a, q0, negm);
      s = MFMA32(*(const bf16x8*)(a + 1024), q1, s);
#pragma unroll
      for (int r = 0; r < 16; ++r) s[r] = __builtin_amdgcn_exp2f(s[r]);
      PK4H(0, s, 0); PK4H(1, s, 8);
      o = MFMA32(*(const bf16x8*)(a + 2048), PLO, o);
      o = MFMA32(*(const bf16x8*)(a + 3072), PHI, o);
      l = LSUM(P, l);
    }
    LQ();
  }
  const __amdgpu_buffer_rsrc_t ors = __builtin_amdgcn_make_buffer_rsrc(ws + WS_O, 0, 4 << 20, 0x00020000);
  gu32* cnt = (gu32*)(ws + WS_CNT);
  {
    const float inv = 1.0f / lq;
    const unsigned oo = (tb * 64 + head * 2) * 1024 + vl;
    u32x4 oa = __builtin_bit_cast(u32x4, pack8(o, 0, inv)), ob2 = __builtin_bit_cast(u32x4, pack8(o, 1, inv));
    PIN(oa); PIN(ob2);
    __builtin_amdgcn_raw_buffer_store_b128(oa, ors, oo, 0, 16);
    __builtin_amdgcn_raw_buffer_store_b128(ob2, ors, oo + 1024, 0, 16);
    WAITV(0);
    STR(2);
    KBL(15);
    STR(3);
  }
  const int tw = wid & 3, kh = (wid >> 2) ^ kflip;
  if (wid == 0) {
    if (lane == 0) __hip_atomic_fetch_add(cnt + qb * 64, 1u, __ATOMIC_RELAXED, __HIP_MEMORY_SCOPE_AGENT);
#pragma unroll 1
    for (int it = 0; it < (1 << 22); ++it) {
      const unsigned v = __hip_atomic_load(cnt + (mt >> 1) * 64, __ATOMIC_RELAXED, __HIP_MEMORY_SCOPE_AGENT);
      if ((unsigned)__builtin_amdgcn_readfirstlane(v) >= 32u) break;
      __builtin_amdgcn_s_sleep(2);
    }
  }
  KB(16);
  STR(5);
  const unsigned ob = (mt * 4 + tw) * 65536 + kh * 32768;
  constexpr int OD = OP_OD;
  bf16x8 of[OD];
  auto ldo = [&](int j) {
    of[j % OD] = __builtin_bit_cast(bf16x8, __builtin_amdgcn_raw_buffer_load_b128(ors, vl + (j & 3) * 1024, ob + (((j >> 2) + g0) & 7) * 4096, OP_AUX));
  };
#pragma unroll
  for (int j = 0; j < OD; ++j) { ldo(j); SBAR(); }
  const int hi = lane >> 5, q4 = (lane & 15) >> 2;
  const unsigned wl = (hi * 4 + q4) * 128 + ((q4 >> 1) * 64) + ((lane >> 4) & 1) * 32 + (lane & 3) * 8;
  const unsigned wbE = lds0 + K2_WE + kh * 16384, wbM = lds0 + K2_CV + kh * 32768, wbL = lds0 + kh * K2_STAGE;
  unsigned wa0 = wbE + wl, wa1 = wbE + (wl ^ 64);
  f32x16 acc0 = (f32x16){}, acc1 = (f32x16){};
  bf16x8 w0[3], w1[3];
  auto ldw = [&](int j) {
    if (j == 8) { wa0 += wbM - wbE; wa1 += wbM - wbE; }
    if (j == 24) { wa0 += wbL - wbM; wa1 += wbL - wbM; }
    const unsigned a = (j < 8 ? j : j < 24 ? j - 8 : j - 24) * 2048;
    w0[j % 3] = cat44(lds_tr(wa0 + a), lds_tr(wa0 + a + 1024));
    w1[j % 3] = cat44(lds_tr(wa1 + a), lds_tr(wa1 + a + 1024));
  };
  STR(6);
  ldw(0); ldw(1);
  SBAR();
#pragma unroll
  for (int j = 0; j < 32; ++j) {
    if (j == 1) STR(7);
    if (j + 2 < 32) ldw(j + 2);
    acc0 = MFMA32(w0[j % 3], of[j % OD], acc0);
    acc1 = MFMA32(w1[j % 3], of[j % OD], acc1);
    if (j + OD < 32) ldo(j + OD);
    SBAR();
  }
  STR(10);
  {
    const f32x16 snd = kh ? acc0 : acc1;
    const unsigned ra = lds0 + 32768 + wid * 4096 + vl;
#pragma unroll
    for (int i = 0; i < 4; ++i)
      *(LAS f32x4*)(uintptr_t)(ra + i * 1024) = (f32x4){snd[4 * i], snd[4 * i + 1], snd[4 * i + 2], snd[4 * i + 3]};
    KBL(17);
    STR(11);
    const f32x16 kp = kh ? acc1 : acc0;
    const unsigned rb = lds0 + 32768 + (wid ^ 4) * 4096 + vl;
    unsigned short* od = out + (size_t)((mt * 4 + tw) * 32 + (lane & 31)) * 2048 + 1024 + nt * 64 + kh * 32 + hi * 8;
    u32x2 a[4];
#pragma unroll
    for (int i = 0; i < 4; ++i) {
      const f32x4 r = *(const LAS f32x4*)(uintptr_t)(rb + i * 1024);
      a[i] = (u32x2){pk2(kp[4 * i] + r[0], kp[4 * i + 1] + r[1]), pk2(kp[4 * i + 2] + r[2], kp[4 * i + 3] + r[3])};
    }
#pragma unroll
    for (int i = 0; i < 2; ++i) {
      const auto x0 = __builtin_amdgcn_permlane32_swap(a[2 * i][0], a[2 * i + 1][0], false, false);
      const auto x1 = __builtin_amdgcn_permlane32_swap(a[2 * i][1], a[2 * i + 1][1], false, false);
      st16b(od + i * 16, (u32x4){x0[0], x1[0], x0[1], x1[1]});
    }
  }
  STR(12);
  TPRE(18);
  TB_OUT();
}

extern "C" void kernel_launch(void* const* d_in, const int* in_sizes, int n_in, void* d_out, int out_size,
                              void* d_ws, size_t ws_size, hipStream_t stream) {
  const unsigned short* aq = (const unsigned short*)d_in[0];
  const unsigned short* ak = (const unsigned short*)d_in[1];
  const unsigned short* av = (const unsigned short*)d_in[2];
  const unsigned short* shk = (const unsigned short*)d_in[3];
  const unsigned short* ssk = (const unsigned short*)d_in[4];
  const unsigned short* ssd = (const unsigned short*)d_in[5];
  const unsigned short* x = (const unsigned short*)d_in[6];
  const unsigned short* wqkv = (const unsigned short*)d_in[7];
  const unsigned short* wo = (const unsigned short*)d_in[8];
  char* ws = (char*)d_ws;
  unsigned short* out = (unsigned short*)d_out;
  (void)hipFuncSetAttribute((const void*)k1_qkv, hipFuncAttributeMaxDynamicSharedMemorySize, K1_LDS);
  (void)hipFuncSetAttribute((const void*)k2_attn_conv, hipFuncAttributeMaxDynamicSharedMemorySize, K2_LDS);
  k1_qkv<<<256, 768, K1_LDS, stream>>>(x, wqkv, ak, av, shk, ws);
  k2_attn_conv<<<256, 768, K2_LDS, stream>>>(aq, ssk, ssd, wo, ws, out);
}
```

```cpp
#include <hip/hip_runtime.h>
#include <hip/hip_bf16.h>
#include <stdint.h>

typedef short bf16x8 __attribute__((ext_vector_type(8)));
typedef short s16x4 __attribute__((ext_vector_type(4)));
typedef float f32x16 __attribute__((ext_vector_type(16)));
typedef float f32x4 __attribute__((ext_vector_type(4)));
typedef unsigned u32x4 __attribute__((ext_vector_type(4)));
typedef unsigned u32x2 __attribute__((ext_vector_type(2)));
typedef __bf16 bf16v2 __attribute__((ext_vector_type(2)));
typedef float f32x2 __attribute__((ext_vector_type(2)));
typedef unsigned u32x8 __attribute__((ext_vector_type(8)));
typedef __bf16 bf16v8 __attribute__((ext_vector_type(8)));
typedef __bf16 bf16v16 __attribute__((ext_vector_type(16)));
#define LAS __attribute__((address_space(3)))
#define GAS __attribute__((address_space(1)))
#define DI __device__ __forceinline__
#define MFMA32(a, b, c) __builtin_amdgcn_mfma_f32_32x32x16_bf16((a), (b), (c), 0, 0, 0)
#define MFMA16(a, b, c) __builtin_amdgcn_mfma_f32_16x16x32_bf16((a), (b), (c), 0, 0, 0)

constexpr size_t WS_Q = 0;
constexpr size_t WS_KV = WS_Q + (4u << 20);
constexpr size_t WS_O = WS_KV + (8u << 20);
constexpr size_t WS_UR = WS_O + (4u << 20);
constexpr size_t WS_KMAX = WS_UR + (4u << 20);
constexpr size_t WS_QMAX = WS_KMAX + 4096;
constexpr size_t WS_CNT = WS_QMAX + 4096;
#define NREP 8
#define PSL 2

DI unsigned pk2(float lo, float hi) {
  f32x2 v = {lo, hi};
  bf16v2 b = __builtin_convertvector(v, bf16v2);
  return __builtin_bit_cast(unsigned, b);
}
DI float bflo(unsigned u) { return __uint_as_float(u << 16); }
DI float bfhi(unsigned u) { return __uint_as_float(u & 0xffff0000u); }
DI float sq2(unsigned u, float c) {
  const bf16v2 b = __builtin_bit_cast(bf16v2, u);
  return __builtin_amdgcn_fdot2_f32_bf16(b, b, c, false);
}
DI bf16x8 pack8(const f32x16& x, int s, float scale) {
  u32x4 p;
  p[0] = pk2(x[8 * s + 0] * scale, x[8 * s + 1] * scale);
  p[1] = pk2(x[8 * s + 2] * scale, x[8 * s + 3] * scale);
  p[2] = pk2(x[8 * s + 4] * scale, x[8 * s + 5] * scale);
  p[3] = pk2(x[8 * s + 6] * scale, x[8 * s + 7] * scale);
  return __builtin_bit_cast(bf16x8, p);
}
DI unsigned code_touch(int wid, unsigned off) {
  unsigned v;
  unsigned long long pc;
  asm volatile("s_getpc_b64 %1\n\ts_cmp_eq_u32 %2, 0\n\ts_cselect_b64 exec, -1, 0\n\t"
               "global_load_dword %0, %3, %1\n\ts_mov_b64 exec, -1" : "=&v"(v), "=&s"(pc) : "s"(wid), "v"(off) : "scc");
  return v;
}
DI bf16x8 lds_rd128(unsigned a) { return *(const LAS bf16x8*)(uintptr_t)a; }
DI s16x4 lds_tr(unsigned a) {
  typedef short v4i16_t __attribute__((ext_vector_type(4)));
  return __builtin_amdgcn_ds_read_tr16_b64_v4i16((LAS v4i16_t*)(uintptr_t)a);
}
DI bf16x8 cat44(s16x4 lo, s16x4 hi) { return __builtin_shufflevector(lo, hi, 0, 1, 2, 3, 4, 5, 6, 7); }
#define SBAR() __builtin_amdgcn_sched_barrier(0)
#define WAITV(n) asm volatile("s_waitcnt vmcnt(" #n ")" ::: "memory")
#define HOLD4(x) asm volatile("s_nop 1" :: "v"((x)[0]), "v"((x)[1]), "v"((x)[2]), "v"((x)[3]))
#define BARRIER_L() asm volatile("s_waitcnt lgkmcnt(0)\n\ts_barrier" ::: "memory")
#define BARRIER() asm volatile("s_barrier" ::: "memory")
#define KB(i) BARRIER()
#define KBL(i) BARRIER_L()
template <int V> struct Ic { static constexpr int v = V; };
template <int A, int B, class F> DI void sfor(F&& f) {
  if constexpr (A < B) { f(Ic<A>{}); sfor<A + 1, B>(f); }
}
#define AI __attribute__((always_inline))
DI float xhalf_max(float v) {
  auto r = __builtin_amdgcn_permlane32_swap(__float_as_uint(v), __float_as_uint(v), false, false);
  return fmaxf(__uint_as_float(r[0]), __uint_as_float(r[1]));
}

constexpr int K1_STAGE = 40960, K1_LDS = 4 * K1_STAGE, K2_LDS = 163840;
constexpr float QSCALE = 0.17677669529663687f * 1.4426950408889634f;
DI void st16(void* p, u32x4 v) {
  asm volatile("global_store_dwordx4 %0, %1, off sc1\n\ts_nop 1" :: "v"(p), "v"(v) : "memory");
}

__global__ __launch_bounds__(768, 3) void k1_qkv(const unsigned short* x, const unsigned short* w,
                                                 const unsigned short* ak, const unsigned short* av,
                                                 const unsigned short* shk, char* ws
                                                 ) {
  extern __shared__ __attribute__((aligned(16))) char lds[];
  const unsigned lds0 = (unsigned)(uintptr_t)(LAS char*)lds;
  const int tid = threadIdx.x, lane = tid & 63;
  const int wid = __builtin_amdgcn_readfirstlane(tid >> 6);
  const int bx = blockIdx.x, xg = bx & 7, li = bx >> 3;
  const int mt = 8 * (xg & 1) + (li & 7), nt = 4 * (xg >> 1) + (li >> 3);
#define K1_SK 5
#define K1_SV 10
  const int ntk = (nt + K1_SK) & 15, ntv = (nt + K1_SV) & 15;
#define K1_S0 ((li & 7) + (li >> 3))
  const int s0 = K1_S0;
#define K1_WIN 4
#define K1_XR ((xg >> 1) + 2 * xg)
  const int w0 = (K1_XR) * K1_WIN;
#define CP1 21
  unsigned pfv = 0u;
  if (CP1) pfv = code_touch(wid, min((li & 7) * 4096 + lane * 64, CP1 * 1024 - 64));
  if (__builtin_expect((wid & 12) == 4, 1)) {
    const int lw = wid - 4;
    const unsigned rowl = lane >> 3, sl = lane & 7;
    unsigned vx[4], vw[6];
    vx[0] = (mt * 128 + lw * 32 + rowl) * 2048 + ((sl ^ (lane >> 4)) * 16);
    vw[0] = rowl * 6144 + (lw * 98304 + nt * 128) + ((sl ^ (rowl & 2)) * 16);
    asm volatile("" : "+v"(vx[0]), "+v"(vw[0]));
    vx[1] = (vx[0] ^ 64) + 15360; vx[2] = vx[0] + 2 * 15360; vx[3] = vx[1] + 2 * 15360;
    vw[1] = (vw[0] ^ 64) + 48128;
#pragma unroll
    for (int i = 2; i < 6; ++i) vw[i] = vw[i & 1] + ((i >> 1) * 2048 + ((i >> 1) == 1 ? ntk - nt : ntv - nt) * 128);
    asm volatile("" : "+v"(vx[1]), "+v"(vx[2]), "+v"(vx[3]), "+v"(vw[1]), "+v"(vw[2]), "+v"(vw[3]), "+v"(vw[4]), "+v"(vw[5])
                 : "s"(ak), "s"(av), "s"(shk), "s"(ws));
    const char* xb = (const char*)x;
    const char* wb = (const char*)w;
    auto issx = [&](int s, int buf) {
      const int ks = ((s + w0) & (16 - K1_WIN)) | ((s + s0) & (K1_WIN - 1));
      asm volatile("s_mov_b32 m0, %0\n\ts_nop 0\n\t"
                   "global_load_lds_dwordx4 %2, %1\n\tglobal_load_lds_dwordx4 %3, %1 offset:1024\n\t"
                   "global_load_lds_dwordx4 %4, %1 offset:2048\n\tglobal_load_lds_dwordx4 %5, %1 offset:3072"
                   :: "s"(lds0 + buf * K1_STAGE + lw * 4096), "s"(xb + ks * 128), "v"(vx[0]), "v"(vx[1]), "v"(vx[2]), "v"(vx[3]) : "memory");
    };
    auto issw = [&](int s, int buf) {
      const int ks = ((s + w0) & (16 - K1_WIN)) | ((s + s0) & (K1_WIN - 1));
      const unsigned dst = lds0 + buf * K1_STAGE + 16384 + lw * 2048;
      asm volatile("s_mov_b32 m0, %0\n\ts_nop 0\n\t"
                   "global_load_lds_dwordx4 %2, %1\n\tglobal_load_lds_dwordx4 %3, %1 offset:1024\n\t"
                   "s_add_u32 m0, %0, 0x2000\n\ts_nop 0\n\t"
                   "global_load_lds_dwordx4 %4, %1\n\tglobal_load_lds_dwordx4 %5, %1 offset:1024\n\t"
                   "s_add_u32 m0, %0, 0x4000\n\ts_nop 0\n\t"
                   "global_load_lds_dwordx4 %6, %1\n\tglobal_load_lds_dwordx4 %7, %1 offset:1024"
                   :: "s"(dst), "s"(wb + (size_t)ks * 64 * 6144), "v"(vw[0]), "v"(vw[1]), "v"(vw[2]), "v"(vw[3]), "v"(vw[4]), "v"(vw[5])
                   : "memory", "scc");
    };
    auto issue = [&](int s, int buf) { issx(s, buf); issw(s, buf); };
    issue(0, 0);
    issue(1, 1);
    WAITV(10);
    BARRIER();
    issue(2, 2); issue(3, 3);
    for (int s = 0; s < 12; ++s) {
      WAITV(20); BARRIER();
      issue(s + 4, s & 3);
    }
    WAITV(20); BARRIER();
    WAITV(10); BARRIER();
    WAITV(0); BARRIER();
    return;
  }
  if (wid >= 8) {
    const int cg = lane & 7, pr = (lane >> 4) & 1, pg = (lane >> 5) * 2 + ((lane >> 3) & 1);
    const int pcb = bx & 15, prb = bx >> 4;
    const int tb = prb * 4 + (wid - 8);
    const int ch0 = pcb * 64 + cg * 8, t0 = tb * 32 + pg * 8 + pr * 4;
    u32x4 kr[7], vr[4], wk[4];
    char* ur = ws + WS_UR + (pcb * 64 + tb) * 4096 + lane * 16;
    const unsigned ut = lds0 + (wid - 8) * 4096 + cg * 128 + ((pr * 4 + 3 - pg) ^ cg) * 16;
    const unsigned ug = lds0 + (wid - 8) * 4096 + (lane >> 3) * 128 + ((lane ^ (lane >> 3)) & 7) * 16;
    if (bx == 0 && tid - 512 < 8 * NREP + 16) ((unsigned*)(ws + WS_CNT))[(tid - 512) * 64] = 0u;
    auto ldk = [&](int r) { const int t = t0 - 3 + r; kr[r] = *(const u32x4*)(ak + (size_t)(t < 0 ? 0 : t) * 1024 + ch0); };
    auto ldv = [&](int r) { vr[r] = *(const u32x4*)(av + (size_t)(t0 + r) * 1024 + ch0); };
    auto ldw = [&](int r) { wk[r] = *(const u32x4*)(shk + (size_t)ch0 * 4 + r * 8); };
#define PL0 11
#define PLN 6
    auto ld = [&](int i) { if (i < 7) ldk(i); else if (i < 11) ldv(i - 7); };
    ldw(0); ldw(1); ldw(2); ldw(3);
    auto cmp = [&](int j) {
      const f32x2 w0 = {bflo(wk[j][0]), bflo(wk[j][2])}, w1 = {bfhi(wk[j][0]), bfhi(wk[j][2])};
      const f32x2 w2 = {bflo(wk[j][1]), bflo(wk[j][3])}, w3 = {bfhi(wk[j][1]), bfhi(wk[j][3])};
      f32x2 kf[7], u[4];
#pragma unroll
      for (int r = 0; r < 7; ++r) kf[r] = (f32x2){bflo(kr[r][j]), bfhi(kr[r][j])};
#pragma unroll
      for (int r = 0; r < 4; ++r) {
        const f32x2 a = __builtin_elementwise_fma(kf[r + 3], w0, __builtin_elementwise_fma(kf[r + 2], w1,
                        __builtin_elementwise_fma(kf[r + 1], w2, kf[r] * w3)));
        u[r] = a * (f32x2){bflo(vr[r][j]), bfhi(vr[r][j])};
      }
      const auto s0 = __builtin_amdgcn_permlane16_swap(pk2(u[3][0], u[2][0]), pk2(u[3][1], u[2][1]), false, false);
      const auto s1 = __builtin_amdgcn_permlane16_swap(pk2(u[1][0], u[0][0]), pk2(u[1][1], u[0][1]), false, false);
      *(LAS u32x4*)(uintptr_t)(ut + j * 1024) = (u32x4){s0[1], s1[1], s0[0], s1[0]};
    };
#pragma unroll
    for (int b = 0; b < 16; ++b) {
      SBAR(); BARRIER(); SBAR();
#pragma unroll
      for (int i = 0; i < PLN; ++i) if (b >= PL0) ld((b - PL0) * PLN + i);
      if (b == 15) {
        if (t0 == 0) { kr[0] = (u32x4){0u, 0u, 0u, 0u}; kr[1] = kr[0]; kr[2] = kr[0]; }
#pragma unroll
        for (int c = 0; c < 4; ++c) cmp(c);
        u32x4 t[4];
#pragma unroll
        for (int c = 0; c < 4; ++c) t[c] = *(const LAS u32x4*)(uintptr_t)(ug + c * 1024);
#pragma unroll
        for (int c = 0; c < 4; ++c) st16(ur + c * 1024, t[c]);
      }
    }
    return;
  }
  const int wm = wid & 1, wn = wid >> 1;
  const int c16 = lane & 15, g = lane >> 4, q4 = c16 >> 2;
  const unsigned xa = lds0 + (wm * 64 + c16) * 128 + ((g ^ (c16 >> 1)) * 16);
  const unsigned wa = lds0 + 16384 + (8 * g + q4) * 128 + (((2 * wn) ^ ((q4 >> 1) | ((g & 1) << 1))) * 32) + (lane & 3) * 8;
  f32x4 acc[4][6];
#pragma unroll
  for (int i = 0; i < 4; ++i)
#pragma unroll
    for (int j = 0; j < 6; ++j) acc[i][j] = (f32x4){};
  bf16x8 xf[2][4];
  unsigned xa0 = xa, xa1 = xa ^ 64, wa0 = wa, wa1 = wa ^ 32;
  auto ldx = [&](int kk, int i) { xf[kk][i] = lds_rd128((kk ? xa1 : xa0) + i * 2048); };
  const __amdgpu_buffer_rsrc_t wrs = __builtin_amdgcn_make_buffer_rsrc(ws, 0, WS_O, 0x00020000);
  unsigned vo1 = g * 512 + c16 * 16, vo2 = g * 512 + ((c16 & 7) + 16 * (c16 >> 3)) * 16;
  unsigned soq = WS_Q + ((nt * 2 + wn) * 64 + mt * 4 + wm * 2) * 2048, sok = WS_KV + ((ntk * 2 + wn) * 64 + mt * 4 + wm * 2) * 4096;
  unsigned sov = WS_KV + ((ntv * 2 + wn) * 64 + mt * 4 + wm * 2) * 4096;
  unsigned sok2 = sok + 4096, sov2 = sov + 4096;
  unsigned vo3 = WS_KMAX + ((lane ? 1024 + nt * 64 : ntk * 64) + wn * 32 + mt * 2 + wm) * 4;
  asm volatile("" : "+v"(vo1), "+v"(vo2), "+v"(vo3));
  asm volatile("" : "+s"(soq), "+s"(sok), "+s"(sov), "+s"(sok2), "+s"(sov2));
#pragma unroll
  for (int i = 0; i < 4; ++i) asm volatile("" : "+v"(acc[i][2]), "+v"(acc[i][3]), "+v"(acc[i][4]), "+v"(acc[i][5]));
  asm volatile("" : "+v"(xa0), "+v"(xa1), "+v"(wa0), "+v"(wa1));
  __builtin_amdgcn_s_waitcnt(0xc07f);
#define AL0 4
  SBAR();
  asm volatile(".p2align 5\n\t.fill %1, 4, 0xbf800000\n\ts_waitcnt vmcnt(0)" :: "v"(pfv), "n"(AL0));
  BARRIER();
  s16x4 wh[12][2];
  auto lw2 = [&](int j, int q) {
    const int kk = q >> 1;
    wh[2 * j + kk][q & 1] = lds_tr(((j & 1) ? wa1 : wa0) + (j >> 1) * 8192 + kk * 4096 + (q & 1) * 512);
    SBAR();
  };
  auto lx2 = [&](int kk, int i) { ldx(kk, i); SBAR(); };
  auto m1 = [&](int i, int j, int kk) {
    const bf16x8 wv = cat44(wh[2 * j + kk][0], wh[2 * j + kk][1]);
    if (j < 4) acc[i][j] = MFMA16(wv, xf[kk][i], acc[i][j]); else acc[i][j] = MFMA16(xf[kk][i], wv, acc[i][j]);
    asm volatile("" : "+v"(acc[i][j]));
    SBAR();
  };
#define K1_TILES 00, 010, 020, 030, 01, 011, 021, 031, 02, 012, 022, 032, 03, 013, 023, 033, 04, 05, 014, 015, 024, 025, 034, 035
#define K1_KF 0x99aaaa
#define K1_WR 32, 33, 36, 37, 44, 45, 0, 1, 2, 3, 4, 5, 8, 9, 10, 11, 18, 19, 16, 17, 24, 25, 22, 23
#define K1_AS 28
#define K1_BP 32
#define K1_XS 34, 35, 38, 39, 42, 43, 46, 47
  static constexpr int tl[24] = {K1_TILES}, wr[24] = {K1_WR}, xr[8] = {K1_XS};
#define KOF(t) (((K1_KF >> ((t) >> 1)) ^ (t)) & 1)
  auto rds = [&](auto T) AI {
    constexpr int t = T.v;
    sfor<0, 8>([&](auto C) AI { constexpr int f = 34 + (C.v >> 1) * 4 + (C.v & 1); if constexpr (xr[C.v] == t) lx2(KOF(f), tl[f >> 1] >> 3); });
    sfor<0, 24>([&](auto C) AI { if constexpr (wr[C.v] == t) lw2(C.v >> 2, C.v & 3); });
  };
  auto ms = [&](auto T) AI { constexpr int t = T.v; m1(tl[t >> 1] >> 3, tl[t >> 1] & 7, KOF(t)); };
  sfor<32, 48>(rds);
  unsigned so = 0;
  SBAR();
  unsigned dd;
#define K1_R0 15
  sfor<0, K1_R0>([&](auto T) AI { ms(T); rds(T); });
  for (int s = 0; s < 15; ++s)
    sfor<K1_R0, K1_R0 + 48>([&](auto U) AI {
      constexpr int t = U.v % 48;
      if (t == K1_BP) { __builtin_amdgcn_s_waitcnt(0xc07f); BARRIER(); SBAR(); }
      ms(Ic<t>{}); rds(Ic<t>{});
      if (t == K1_AS - 8) { dd = (so == 3 * K1_STAGE) ? (unsigned)(-3 * K1_STAGE) : (unsigned)K1_STAGE; asm volatile("" : "+s"(dd)); SBAR(); }
      if (t == K1_AS - 7) { so += dd; xa0 += dd; asm volatile("" : "+s"(so), "+v"(xa0)); SBAR(); }
      if (t == K1_AS) { xa1 += dd; asm volatile("" : "+v"(xa1)); SBAR(); }
      if (t == K1_AS + 1) { wa0 += dd; asm volatile("" : "+v"(wa0)); SBAR(); }
      if (t == K1_AS + 2) { wa1 += dd; asm volatile("" : "+v"(wa1)); SBAR(); }
    });
  {
    u32x4 qp[4], kp[4], vp[4];
    auto stk = [&](int i) {
      kp[i] = (u32x4){pk2(acc[i][2][0], acc[i][2][1]), pk2(acc[i][2][2], acc[i][2][3]),
                      pk2(acc[i][3][0], acc[i][3][1]), pk2(acc[i][3][2], acc[i][3][3])};
      __builtin_amdgcn_raw_buffer_store_b128(kp[i], wrs, vo2 + (i & 1) * 128, (i >> 1) ? sok2 : sok, 16);
    };
    auto stv = [&](int i) {
      const f32x4 va = acc[i & 2][4 + (i & 1)], vb = acc[(i & 2) + 1][4 + (i & 1)];
      vp[i] = (u32x4){pk2(va[0], va[1]), pk2(va[2], va[3]), pk2(vb[0], vb[1]), pk2(vb[2], vb[3])};
      __builtin_amdgcn_raw_buffer_store_b128(vp[i], wrs, vo1 + 2048 + (i & 1) * 256, (i >> 1) ? sov2 : sov, 16);
    };
    float nq_[4];
    auto ssq = [&](const u32x4& p) { return sq2(p[3], sq2(p[2], sq2(p[1], sq2(p[0], 0.f)))); };
    SBAR();
    auto hq = [&](int i, int h) {
      float m4[4];
#pragma unroll
      for (int e = 0; e < 4; ++e) { m4[e] = acc[i][h][e] * QSCALE; asm volatile("" : "+v"(m4[e])); }
      qp[i][2 * h] = pk2(m4[0], m4[1]); qp[i][2 * h + 1] = pk2(m4[2], m4[3]);
      if (h) __builtin_amdgcn_raw_buffer_store_b128(qp[i], wrs, vo1 + (i >> 1) * 2048 + (i & 1) * 256, soq, 16);
      SBAR();
    };
    float nk_[4];
    sfor<K1_R0, 48>([&](auto T) AI {
      constexpr int t = T.v;
      constexpr int q0 = 16;
      ms(T);
      if constexpr (t < 32) rds(T);
      if constexpr (t >= q0 && t < q0 + 8) hq((t - q0) >> 1, (t - q0) & 1);
      if constexpr (t >= 28 && t < 32) { nq_[t - 28] = ssq(qp[t - 28]); SBAR(); }
      if constexpr (t >= 32 && t < 36) { stk(t - 32); SBAR(); }
      if constexpr (t >= 36 && t < 40) { nk_[t - 36] = ssq(kp[t - 36]); SBAR(); }
      if constexpr (t == 41) { stv(0); SBAR(); }
      if constexpr (t == 43) { stv(1); SBAR(); }
    });
    SBAR();
    stv(2); SBAR(); stv(3); SBAR();
    HOLD4(qp); HOLD4(vp); HOLD4(kp); SBAR();
    {
      float h[4];
#pragma unroll
      for (int i = 0; i < 4; ++i) {
        const auto r = __builtin_amdgcn_permlane16_swap(__float_as_uint(nk_[i]), __float_as_uint(nq_[i]), false, false);
        h[i] = __uint_as_float(r[0]) + __uint_as_float(r[1]);
      }
      const auto r0 = __builtin_amdgcn_permlane32_swap(__float_as_uint(h[0]), __float_as_uint(h[1]), false, false);
      const auto r1 = __builtin_amdgcn_permlane32_swap(__float_as_uint(h[2]), __float_as_uint(h[3]), false, false);
      unsigned m = max(__float_as_uint(__uint_as_float(r0[0]) + __uint_as_float(r0[1])), __float_as_uint(__uint_as_float(r1[0]) + __uint_as_float(r1[1])));
      m = max(m, (unsigned)__builtin_amdgcn_update_dpp(0, (int)m, 0x128, 0xf, 0xf, false));
      m = max(m, (unsigned)__builtin_amdgcn_update_dpp(0, (int)m, 0x124, 0xf, 0xf, false));
      m = max(m, (unsigned)__builtin_amdgcn_update_dpp(0, (int)m, 0x122, 0xf, 0xf, false));
      m = max(m, (unsigned)__builtin_amdgcn_update_dpp(0, (int)m, 0x121, 0xf, 0xf, false));
      const auto r2 = __builtin_amdgcn_permlane32_swap(m, m, false, false);
      m = max(r2[0], r2[1]);
      asm volatile("s_mov_b64 exec, 0x10001\n\tglobal_store_dword %0, %1, %2\n\ts_mov_b64 exec, -1" :: "v"(vo3), "v"(m), "s"(ws) : "memory");
    }
  }
}

constexpr int K2_STAGE = 16384;
constexpr int K2_WE = 65536;
constexpr int K2_CV = 98304, CV_SZ = 16384;
constexpr int CV_URE = 0, CV_URO = 4160, CV_HP = 8320, CV_Z = 0;
typedef __attribute__((address_space(1))) unsigned gu32;

DI void glds4(const void* gbase, unsigned v0, unsigned v1, unsigned v2, unsigned v3, unsigned lds_dst) {
  asm volatile("s_mov_b32 m0, %5\n\ts_nop 0\n\t"
               "global_load_lds_dwordx4 %1, %0\n\tglobal_load_lds_dwordx4 %2, %0 offset:1024\n\t"
               "global_load_lds_dwordx4 %3, %0 offset:2048\n\tglobal_load_lds_dwordx4 %4, %0 offset:3072"
               :: "s"(gbase), "v"(v0), "v"(v1), "v"(v2), "v"(v3), "s"(lds_dst) : "memory");
}

#define APT 0x2110
#define APRI(t) do { __builtin_amdgcn_s_setprio((APT >> (12 - 4 * (t))) & 3); SBAR(); } while (0)
#define HP0 3
#define HPC 0
#define B_AUX 2
#define A_AUX 0
#define WBM 31
#define WBD 0

__global__ __launch_bounds__(768, 3) void k2_attn_conv(const unsigned short* aq, const unsigned short* ssk,
                                                       const unsigned short* ssd, const unsigned short* wo,
                                                       char* ws_, unsigned short* out_) {
  extern __shared__ __attribute__((aligned(16))) char lds[];
  u32x4 ka;
  asm volatile("s_load_dwordx4 %0, %1, 0x20" : "=s"(ka) : "s"(__builtin_amdgcn_kernarg_segment_ptr()));
  SBAR();
  const unsigned lds0 = (unsigned)(uintptr_t)(LAS char*)lds;
  const int tid = threadIdx.x, lane = tid & 63;
  const int wid = __builtin_amdgcn_readfirstlane(tid >> 6);
  const int bx = blockIdx.x, xg = bx & 7, li = bx >> 3;
  const int head = xg * 4 + (li >> 3), qb = li & 7;
  const int mt = 2 * xg + (li & 1), nt = li >> 1;
  unsigned pfv = 0u;
  const unsigned vl = lane * 16;
  const int tb = qb * 8 + wid;
  unsigned qoff = vl + WS_Q + (head * 64 + tb) * 2048, kvoff = vl + WS_KV + head * 262144 + wid * 4096;
  unsigned kmoff = WS_KMAX + head * 128;
  asm volatile("s_waitcnt lgkmcnt(0)" : "+v"(qoff), "+v"(kvoff), "+s"(kmoff), "+s"(ka));
  char* const ws = (char*)(GAS char*)(((unsigned long long)ka[1] << 32) | ka[0]);
  unsigned short* const out = (unsigned short*)(GAS unsigned short*)(((unsigned long long)ka[3] << 32) | ka[2]);
  const char* kvg = ws + WS_KV + (size_t)head * 262144;
  const unsigned vwo = (lane >> 3) * 2048 + (((lane & 7) ^ (((lane >> 4) & 1) << 2)) * 16);
  const char* wob = (const char*)(wo + nt * 64);
#define OP_PR 1
#define OP_PSW 16
#define OP_AUX 16
#define OP_P0 5
#define OP_OD 5
  const int g0 = (li >> 3) * 2;
  auto wo4 = [&](int half, int pos, unsigned dst) {
    const int row0 = half * 512 + ((pos + 4 * g0) & 31) * 16;
    glds4(wob + (size_t)row0 * 2048, vwo, vwo + 15360, vwo + 30720, vwo + 46080, dst);
  };
  const __amdgpu_buffer_rsrc_t ors = __builtin_amdgcn_make_buffer_rsrc(ws + WS_O, 0, 4 << 20, 0x00020000);
  gu32* const cnt = (gu32*)(ws + WS_CNT);
  if (wid >= 8) {
    __builtin_amdgcn_s_setprio(HP0);
    const int cw = wid - 8;
    const int chg = xg * 32 + li;
    const int ch = chg * 4 + cw;
    const unsigned cb = lds0 + K2_CV + cw * CV_SZ;
    auto issue = [&](int s) {
      glds4(kvg + (size_t)s * K2_STAGE + cw * 4096, vl, vl, vl, vl, lds0 + (s & 3) * K2_STAGE + cw * 4096);
    };
    u32x4 qw[4];
    const __amdgpu_buffer_rsrc_t qrs = __builtin_amdgcn_make_buffer_rsrc((void*)aq, 0, 4 << 20, 0x00020000);
    const __amdgpu_buffer_rsrc_t ars = __builtin_amdgcn_make_buffer_rsrc(out, 0, 8 << 20, 0x00020000);
    const unsigned vz = (lane >> 3) * 16 * 2048 + (lane & 7) * 16, zr = (li & 14) * 128 + cw * 4 + (li & 1) * 2;
    auto ldq = [&](int i) {
      qw[i] = __builtin_amdgcn_raw_buffer_load_b128(qrs, vz, (zr + (i >> 1) * 128 + (i & 1)) * 2048 + (chg >> 4) * 128, 0);
    };
#define BPS 15, 56, 97, 136, 172, 208, 248
#define WE0 0
#define WAITN(x) asm volatile("s_waitcnt vmcnt(%0)" :: "n"(x) : "memory")
    auto ldur = [&]() {
      const unsigned vu = (64 - (lane >> 2)) * 4096 + (lane & 3) * 16;
      glds4(ws + WS_UR - 4096 + (ch >> 6) * 262144 + ((ch >> 1) & 3) * 1024 + ((ch >> 3) & 7) * 128 + (ch & 1) * 64,
            vu, vu - 66560, vu - 2 * 66560, vu - 3 * 66560, cb + CV_URE);
    };
    const unsigned dd = *(const __attribute__((address_space(4))) unsigned*)(uintptr_t)(ssd + (ch & ~1));
    if (lane < 30) *(LAS u32x4*)(uintptr_t)(cb + CV_HP + lane * 16) = (u32x4){0u, 0u, 0u, 0u};
    if (lane < 4) *(LAS u32x4*)(uintptr_t)(cb + CV_URE + 4096 + lane * 16) = (u32x4){0u, 0u, 0u, 0u};
    if (lane < 4) *(LAS u32x4*)(uintptr_t)(cb + CV_URO + 4096 + lane * 16) = (u32x4){0u, 0u, 0u, 0u};
    KB(0);
    issue(2);
    issue(3);
    ldur();
    glds4(ssk + (size_t)ch * 2048, vl, vl, vl, vl, cb + CV_HP + 480);
    WAITN(12); KB(1);
    issue(4);
#define QLS 0x000111100ull
    auto nq = [](int i) { return (int)((QLS >> (28 - 4 * i)) & 15); };
    auto ldqs = [&](int i) {
      int f = 0;
#pragma unroll
      for (int k = -1; k < i; ++k) f += nq(k);
#pragma unroll
      for (int k = f; k < f + nq(i); ++k) ldq(k);
    };
#define MKS 0x00011110
    auto nm = [](int i) { return i < 0 ? 0 : (int)((MKS >> (28 - 4 * i)) & 15); };
    u32x4 mkv = {~0u, ~0u, ~0u, ~0u};
    auto mark = [&](int k) {
      if (k == 0) asm volatile("" : "+v"(mkv));
      __builtin_amdgcn_raw_buffer_store_b128(mkv, ors, vl + (k & 1) * 1024, ((qb * 8 + cw * 2 + (k >> 1)) * 64 + head * 2) * 1024, 16);
      if (k == 3) { SBAR(); asm volatile("s_nop 1" :: "v"(mkv)); }
    };
    auto marks = [&](int i) {
      int f = 0;
#pragma unroll
      for (int k = 0; k < i; ++k) f += nm(k);
#pragma unroll
      for (int k = f; k < f + nm(i); ++k) mark(k);
    };
    auto nx = [&](int i) { return nq(i) + nm(i) + (i == WE0 || i == WE0 + 1 ? 4 : 0); };
    ldqs(-1);
    asm volatile("s_waitcnt vmcnt(%1)" :: "v"(pfv), "n"(8 + nq(-1)) : "memory");
#pragma unroll
    for (int i = 0; i < 4; ++i) {
      const unsigned a = cb + CV_URE + i * 1024 + lane * 16;
      const u32x4 d = *(const LAS u32x4*)(uintptr_t)a;
      const unsigned nx = *(const LAS unsigned*)(uintptr_t)(a + 16);
      u32x4 o;
      o[0] = __builtin_amdgcn_alignbit(d[1], d[0], 16);
      o[1] = __builtin_amdgcn_alignbit(d[2], d[1], 16);
      o[2] = __builtin_amdgcn_alignbit(d[3], d[2], 16);
      o[3] = __builtin_amdgcn_alignbit(nx, d[3], 16);
      *(LAS u32x4*)(uintptr_t)(cb + CV_URO + i * 1024 + lane * 16) = o;
    }
    const int i16 = lane & 15, g = lane >> 4;
    f32x4 y[8];
    {
      const unsigned dv = (ch & 1) ? dd >> 16 : dd & 0xffffu;
      const unsigned dl = (g == (i16 >> 3)) ? ((i16 & 1) ? dv : dv << 16) : 0u;
      u32x4 da;
#pragma unroll
      for (int e = 0; e < 4; ++e) da[e] = (3 - ((i16 & 7) >> 1) == e) ? dl : 0u;
      unsigned a = cb + CV_URE + (2040 - 1792 - 16 * i16 - 8 * (g & 1)) * 2;
      asm volatile("" : "+v"(a));
#pragma unroll
      for (int n = 0; n < 8; ++n) y[n] = MFMA16(__builtin_bit_cast(bf16x8, da), lds_rd128(a + (7 - n) * 512), (f32x4){});
    }
    unsigned ua = cb + ((i16 & 1) ? CV_URE : CV_URO) + (903 - (i16 >> 1) + 4 * g) * 4 - 7 * 512;
    unsigned ha = cb + CV_HP + i16 * 32 + g * 16;
    bf16x8 uf[8], hf[8];
    auto ldu = [&](int d) {
      u32x4 t;
#pragma unroll
      for (int j = 0; j < 4; ++j) t[j] = *(const LAS unsigned*)(uintptr_t)(ua + (7 - d) * 512 + j * 4);
      uf[d] = __builtin_bit_cast(bf16x8, t);
    };
    auto ldh = [&](int d) { hf[d] = lds_rd128(ha + d * 512); };
#pragma unroll
    for (int d = 0; d < 8; ++d) ldu(d);
    SBAR();
    auto sync = [&](int r) {
      SBAR();
      __builtin_amdgcn_s_setprio(HP0);
      if (r == 0) WAITN(4 + nq(-1)); else WAITN(nx(r - 2) + 4 + nx(r - 1));
      KB(r + 2); issue(r + 5);
      if (r == 4) { SBAR(); SBAR(); }
      ldqs(r);
      SBAR();
      marks(r);
      SBAR();
      if (r == WE0 || r == WE0 + 1) wo4(r - WE0, cw * 2, lds0 + K2_WE + (r - WE0) * 16384 + cw * 4096);
      __builtin_amdgcn_s_setprio(HPC);
      SBAR();
    };
    auto at = [&](int c0, int c1) {
      constexpr int bp[7] = {BPS};
#pragma unroll
      for (int r = 0; r < 7; ++r) if (c0 <= bp[r] && bp[r] < c1) sync(r);
    };
#pragma unroll
    for (int rho = 0; rho < 8; ++rho) {
      if (rho == 0) {
        WAITN(4 + nq(-1));
#pragma unroll
        for (int d = 0; d < 8; ++d) ldh(7 - d);
        __builtin_amdgcn_s_setprio(HPC);
      }
      ua += 64; ha += 64;
      SBAR();
      if (rho < 7) {
#pragma unroll
        for (int p = 7; p >= 1; --p) {
          at(rho * 36 + (7 - p) * (8 - p) / 2, rho * 36 + (8 - p) * (9 - p) / 2);
#pragma unroll
          for (int n = p; n < 8; ++n) y[n] = MFMA16(uf[n - p], hf[p], y[n]);
          SBAR();
          ldh(p);
          SBAR();
        }
#pragma unroll
        for (int n = 0; n < 8; ++n) {
          at(rho * 36 + 28 + n, rho * 36 + 29 + n);
          y[n] = MFMA16(uf[n], hf[0], y[n]);
          SBAR();
          ldu(n);
          SBAR();
        }
        ldh(0);
      } else {
        asm volatile("s_waitcnt lgkmcnt(0)" ::: "memory");
#pragma unroll
        for (int n = 7; n >= -1; --n) {
          if (n >= 0) {
            at(288 - (n + 1) * (n + 2) / 2, 288 - n * (n + 1) / 2);
#pragma unroll
            for (int p = n; p >= 0; --p) y[n] = MFMA16(uf[n - p], hf[p], y[n]);
          }
          SBAR();
          if (n < 7) *(LAS f32x4*)(uintptr_t)(cb + CV_Z + (n + 1) * 1024 + vl) = y[n + 1];
          SBAR();
        }
      }
      SBAR();
    }
    __builtin_amdgcn_s_setprio(HP0);
    SBAR();
#pragma unroll
    for (int i = 0; i < 4; ++i) asm volatile("" : "+v"(qw[i]));
    WAITV(0); KBL(9);
    issue(12);
    f32x4 zz[2][4];
    {
      const unsigned za = lds0 + K2_CV + CV_Z + cw * 1024 + vl;
#pragma unroll
      for (int h = 0; h < 2; ++h)
#pragma unroll
        for (int c = 0; c < 4; ++c) zz[h][c] = *(const LAS f32x4*)(uintptr_t)(za + c * CV_SZ + h * 4096);
    }
#define WSS 0x02110
    auto nw = [](int i) { return (WSS >> (16 - 4 * i)) & 15; };
    auto wos = [&](int i) {
      int f = 0;
#pragma unroll
      for (int k = 0; k < i; ++k) f += nw(k);
#pragma unroll
      for (int k = f; k < f + nw(i); ++k) wo4(cw >> 1, 8 + 2 * k + (cw & 1) * 8, cb + k * 4096);
    };
    auto cn = [&](int i) { return 4 * nw(i); };
    const __amdgpu_buffer_rsrc_t zrs = __builtin_amdgcn_make_buffer_rsrc(ws + WS_UR, 0, 4 << 20, 0x00020000);
    gu32* const zc = cnt + (8 * NREP + (chg >> 4)) * 64;
    u32x4 zw[4];
#pragma unroll
    for (int i = 0; i < 4; ++i) {
      const int h = i >> 1, e = (i & 1) * 2;
      const u32x4 t = {pk2(zz[h][0][e], zz[h][1][e]), pk2(zz[h][2][e], zz[h][3][e]), pk2(zz[h][0][e + 1], zz[h][1][e + 1]), pk2(zz[h][2][e + 1], zz[h][3][e + 1])};
      zw[i] = t;
      __builtin_amdgcn_raw_buffer_store_b128(t, zrs, (lane >> 4) * 4096 + (lane & 8) * 128 + (lane & 7) * 16,
                                             ((chg >> 4) * 16 + (h * 4 + cw) * 2 + (i & 1)) * 16384 + (li & 1) * 2048 + (li & 14) * 64, 16);
    }
    SBAR(); HOLD4(zw);
    SBAR(); SBAR();
    auto zpoll = [&]() {
#pragma unroll 1
      for (int it = 0; it < (1 << 22); ++it) {
        const unsigned v = __hip_atomic_load(zc, __ATOMIC_RELAXED, __HIP_MEMORY_SCOPE_AGENT);
        if ((unsigned)__builtin_amdgcn_readfirstlane(v) >= 16u) break;
        __builtin_amdgcn_s_sleep(PSL);
      }
    };
    auto zld = [&]() {
      asm volatile("buffer_load_dwordx4 %0, %4, %5, %6 offen sc1\n\tbuffer_load_dwordx4 %1, %4, %5, %6 offen offset:%7 sc1\n\t"
                   "buffer_load_dwordx4 %2, %4, %5, %6 offen offset:%8 sc1\n\tbuffer_load_dwordx4 %3, %4, %5, %6 offen offset:%9 sc1"
                   : "=&v"(zw[0]), "=&v"(zw[1]), "=&v"(zw[2]), "=&v"(zw[3])
                   : "v"((lane & 7) * 128 + (lane >> 3) * 16), "s"(zrs), "s"(chg * 16384 + cw * 4096),
                     "n"(2048), "n"(1024), "n"(3072) : "memory");
    };
    auto zst = [&]() {
      u32x4 t[4];
#pragma unroll
      for (int i = 0; i < 4; ++i) {
#pragma unroll
        for (int e = 0; e < 4; ++e) {
          const unsigned z = zw[(i & 2) + (e >> 1)][(i & 1) * 2 + (e & 1)], q = qw[i][e];
          float p0 = bflo(q) * bflo(z), p1 = bfhi(q) * bfhi(z);
          asm volatile("" : "+v"(p0), "+v"(p1));
          t[i][e] = pk2(p0, p1);
        }
        __builtin_amdgcn_raw_buffer_store_b128(t[i], ars, vz + (lane >> 3) * 16 * 2048, (zr + (i >> 1) * 128 + (i & 1)) * 4096 + (chg >> 4) * 128, A_AUX);
      }
      SBAR(); HOLD4(t); SBAR();
    };
#define ZWAIT(n) asm volatile("s_waitcnt vmcnt(%4)" : "+v"(zw[0]), "+v"(zw[1]), "+v"(zw[2]), "+v"(zw[3]) : "n"(n) : "memory")
    KBL(10); issue(13);
    wos(1); SBAR(); SBAR();
    WAITN(cn(0) + 4 + cn(1)); KB(11);
    if (cw == 1 && lane == 0) __hip_atomic_fetch_add(zc, 1u, __ATOMIC_RELAXED, __HIP_MEMORY_SCOPE_AGENT);
    SBAR();
    issue(14);
    wos(2); SBAR(); SBAR();
    WAITN(cn(1) + 4 + cn(2)); KB(12);
    unsigned zv;
    auto zask = [&]() { asm volatile("global_load_dword %0, %1, %2 sc1" : "=v"(zv) : "v"(0), "s"(zc) : "memory"); };
    auto zget = [&]() {
      asm volatile("s_waitcnt vmcnt(4)" : "+v"(zv) :: "memory");
      if (__builtin_expect((unsigned)__builtin_amdgcn_readfirstlane(zv) < 16u, 0)) zpoll();
      SBAR();
      zld();
    };
    issue(15);
    wos(3);
    WAITN(cn(2) + 4 + cn(3));
    KB(13);
    { SBAR(); SBAR(); }
    u32x4 wl_[8];
    auto wor = [&](int half) {
      const int row0 = half * 512 + ((24 + cw * 2 + 4 * g0) & 31) * 16;
      asm volatile("global_load_dwordx4 %0, %4, %8\n\tglobal_load_dwordx4 %1, %5, %8 offset:1024\n\t"
                   "global_load_dwordx4 %2, %6, %8 offset:2048\n\tglobal_load_dwordx4 %3, %7, %8 offset:3072"
                   : "=&v"(wl_[4 * half]), "=&v"(wl_[4 * half + 1]), "=&v"(wl_[4 * half + 2]), "=&v"(wl_[4 * half + 3])
                   : "v"(vwo), "v"(vwo + 15360), "v"(vwo + 30720), "v"(vwo + 46080), "s"(wob + (size_t)row0 * 2048) : "memory");
    };
    zask();
    wor(0);
    zget();
#pragma unroll
    for (int i = 0; i < 2; ++i) *(LAS u32x4*)(uintptr_t)(lds0 + cw * 2048 + i * 1024 + vl) = (u32x4){0u, 0u, 0u, 0u};
    wos(4); SBAR(); SBAR();
    WAITN(4 + cn(3) + 4 + cn(4));
    KBL(14);
    wor(1);
    SBAR(); SBAR();
    ZWAIT(4); zst();
    const bool wbw = cw == 1 && (li & WBM) == WBM;
    if (wbw) asm volatile("s_sleep %0\n\tbuffer_wbl2 sc1" :: "n"(WBD) : "memory");
#define SIG 14
    if (cw == 0) {
      asm volatile("s_sleep %0" :: "n"(SIG) : "memory");
      if (lane < NREP) __hip_atomic_fetch_add(cnt + (qb * NREP + lane) * 64, 1u, __ATOMIC_RELAXED, __HIP_MEMORY_SCOPE_AGENT);
    }
    if (cw == 0) {
#pragma unroll 1
      for (int it = 0; it < (1 << 22); ++it) {
        const unsigned v = __hip_atomic_load(cnt + ((mt >> 1) * NREP + ((nt * 2 + (mt & 1)) & (NREP - 1))) * 64, __ATOMIC_RELAXED, __HIP_MEMORY_SCOPE_AGENT);
        if ((unsigned)__builtin_amdgcn_readfirstlane(v) >= 32u) break;
        __builtin_amdgcn_s_sleep(PSL);
      }
    }
    if (wbw) WAITN(1); else WAITN(0);
#pragma unroll
    for (int i = 0; i < 8; ++i) asm volatile("" : "+v"(wl_[i]));
    KB(15);
#pragma unroll
    for (int i = 0; i < 8; ++i) *(volatile LAS u32x4*)(uintptr_t)(lds0 + (2 + (i >> 2)) * K2_STAGE + cw * 4096 + (i & 3) * 1024 + vl) = wl_[i];
    SBAR();
    *(volatile LAS unsigned*)(uintptr_t)(lds0 + (lane & 7) * 1024 + cw * 16 + 12) = 0u;
    asm volatile("s_waitcnt lgkmcnt(0)" :: "v"(pfv) : "memory");
    return;
  }
  bf16x8 q0, q1;
  typedef unsigned u32x16 __attribute__((ext_vector_type(16)));
  u32x16 kma, kmb;
  glds4(ws, kvoff, kvoff, kvoff, kvoff, lds0 + wid * 4096);
#define QDL 1
  if (wid < 4) asm volatile("s_sleep %0" :: "n"(QDL) : "memory");
  asm volatile("global_load_dwordx4 %0, %4, %6\n\tglobal_load_dwordx4 %1, %4, %6 offset:1024\n\t"
               "s_load_dwordx16 %2, %6, %5 offset:0\n\ts_load_dwordx16 %3, %6, %5 offset:64"
               : "=&v"(q0), "=&v"(q1), "=&s"(kma), "=&s"(kmb) : "v"(qoff), "s"(kmoff), "s"(ws) : "memory");
  unsigned qmu;
  asm volatile("s_load_dword %0, %2, %1" : "=&s"(qmu) : "s"((unsigned)WS_QMAX + head * 128 + (tb >> 1) * 4), "s"(ws) : "memory");
  bf16x8 qa0, qa1;
  asm volatile("global_load_dwordx4 %0, %2, %3\n\tglobal_load_dwordx4 %1, %2, %3 offset:1024"
               : "=&v"(qa0), "=&v"(qa1) : "v"(qoff ^ 2048), "s"(ws) : "memory");
  f32x16 oa = (f32x16){};
  f32x4 la = (f32x4){};
  asm volatile("" : "+v"(oa), "+v"(la));
  SBAR();
  bf16x8 ones;
  {
    const int r = lane & 15;
#pragma unroll
    for (int e = 0; e < 8; ++e)
      ones[e] = ((r & 2) == 0 && ((r >> 2) & 1) == (e >> 2)) ? (short)0x3f80 : (short)0;
  }
  int sidx = (lane & 1) ? 0xEEEE : 0x4444;
#define LSUM(P, l) __builtin_amdgcn_smfmac_f32_16x16x64_bf16(__builtin_bit_cast(bf16v8, ones), __builtin_bit_cast(bf16v16, P), l, sidx, 0, 0)
  f32x16 o = (f32x16){};
  f32x4 l = (f32x4){};
  u32x8 P = (u32x8){};
  f32x16 sB;
  bf16x8 v1 = ones;
  asm volatile("" : "+v"(o), "+v"(l), "+v"(P), "+v"(v1), "+v"(ones), "+v"(sidx));
  SBAR();
  asm volatile("s_waitcnt lgkmcnt(0)" : "+s"(kma), "+s"(kmb), "+s"(qmu));
  unsigned kmu = 0u;
#define SMX(n) "s_max_u32 %0, %0, %" #n "\n\t"
#define SMAX16(k) asm(SMX(1) SMX(2) SMX(3) SMX(4) SMX(5) SMX(6) SMX(7) SMX(8) SMX(9) SMX(10) SMX(11) SMX(12) SMX(13) SMX(14) SMX(15) SMX(16) \
    : "+s"(kmu) : "s"(k[0]), "s"(k[1]), "s"(k[2]), "s"(k[3]), "s"(k[4]), "s"(k[5]), "s"(k[6]), "s"(k[7]), "s"(k[8]), "s"(k[9]), \
      "s"(k[10]), "s"(k[11]), "s"(k[12]), "s"(k[13]), "s"(k[14]), "s"(k[15]) : "scc")
  SMAX16(kma);
  SMAX16(kmb);
  const unsigned lb = lds0 + lane * 16 + ((wid & 1) ? 8192 : 0);
#define LTH 9.094947017729282e-13f
  f32x16 negm;
  {
    const float mb = __builtin_amdgcn_sqrtf(__uint_as_float(qmu) * __uint_as_float(kmu)) * 1.0009765625f;
#pragma unroll
    for (int r = 0; r < 16; ++r) negm[r] = 0.0f - mb;
    asm volatile("" : "+v"(negm));
  }
#define PRO_PR 3
  __builtin_amdgcn_s_setprio(PRO_PR);
#define AL4 4
  asm volatile(".p2align 5\n\t.fill %0, 4, 0xbf800000" :: "n"(AL4));
  asm volatile("s_waitcnt vmcnt(%1)" : "+s"(kmu) : "n"(4) : "memory");
  KB(0);
  bf16x8 k0 = lds_rd128(lb), k1 = lds_rd128(lb + 1024), v0 = lds_rd128(lb + 2048);
  SBAR();
  asm volatile("s_waitcnt vmcnt(0)" : "+v"(q0), "+v"(q1), "+s"(kmu) :: "memory");
  asm volatile("" : "+v"(qa0), "+v"(qa1));
  f32x16 sA = MFMA32(k0, qa0, negm);
  sA = MFMA32(k1, qa1, sA);
#define EXP4(S, i) do { S[i] = __builtin_amdgcn_exp2f(S[i]); S[i + 1] = __builtin_amdgcn_exp2f(S[i + 1]); \
    S[i + 2] = __builtin_amdgcn_exp2f(S[i + 2]); S[i + 3] = __builtin_amdgcn_exp2f(S[i + 3]); } while (0)
#define EXP2(S, i) do { S[i] = __builtin_amdgcn_exp2f(S[i]); S[i + 1] = __builtin_amdgcn_exp2f(S[i + 1]); } while (0)
#define BF(P) __builtin_bit_cast(bf16x8, P)
#define PIN(x) asm volatile("" : "+v"(x))
#define PK4H(h, S, i) do { P[4 * h] = pk2(S[i], S[i + 1]); P[4 * h + 1] = pk2(S[i + 2], S[i + 3]); \
    P[4 * h + 2] = pk2(S[i + 4], S[i + 5]); P[4 * h + 3] = pk2(S[i + 6], S[i + 7]); } while (0)
#define PLO BF(__builtin_shufflevector(P, P, 0, 1, 2, 3))
#define PHI BF(__builtin_shufflevector(P, P, 4, 5, 6, 7))
#define TILEA(a0, a1) do { \
    sB = MFMA32(k0, q0, negm); SBAR(); k0 = lds_rd128(a1); EXP4(sA, 0); PIN(sA); SBAR(); \
    o = MFMA32(v1, PHI, o); SBAR(); v1 = lds_rd128((a0) + 3072); EXP4(sA, 4); PIN(sA); SBAR(); \
    l = LSUM(P, l); SBAR(); EXP2(sA, 8); PIN(sA); SBAR(); \
    sB = MFMA32(k1, q1, sB); SBAR(); k1 = lds_rd128((a1) + 1024); PK4H(0, sA, 0); EXP2(sA, 10); PIN(sA); PIN(P); SBAR(); \
    oa = MFMA32(v0, PLO, oa); SBAR(); EXP4(sA, 12); PIN(sA); SBAR(); \
    PK4H(1, sA, 8); PIN(P); SBAR(); \
  } while (0)
#define TILEB(a1) do { \
    sA = MFMA32(k0, qa0, negm); SBAR(); EXP4(sB, 0); PIN(sB); SBAR(); \
    oa = MFMA32(v1, PHI, oa); SBAR(); EXP4(sB, 4); PIN(sB); SBAR(); \
    la = LSUM(P, la); SBAR(); EXP2(sB, 8); PIN(sB); SBAR(); \
    sA = MFMA32(k1, qa1, sA); SBAR(); PK4H(0, sB, 0); EXP2(sB, 10); PIN(sB); PIN(P); SBAR(); \
    o = MFMA32(v0, PLO, o); SBAR(); v0 = lds_rd128((a1) + 2048); EXP4(sB, 12); PIN(sB); SBAR(); \
    PK4H(1, sB, 8); PIN(P); SBAR(); \
  } while (0)
  SBAR();
  {
    unsigned a = lb, an = lb + K2_STAGE, so = 2 * K2_STAGE;
    APRI(0); TILEA(a, a + 4096);
    APRI(1); TILEB(a + 4096);
    APRI(2); TILEA(a + 4096, an);
    SBAR();
#pragma unroll 1
    do {
      unsigned t;
      asm volatile("s_and_b32 %3, %2, %5\n\tv_mov_b32 %0, %1\n\ts_addk_i32 %2, %6\n\tv_add_u32 %1, %3, %4"
                   : "+v"(a), "+v"(an), "+s"(so), "=&s"(t) : "v"(lb), "n"(3 * K2_STAGE), "n"(K2_STAGE), "v"(sA) : "scc");
      APRI(3); TILEB(a);
      asm volatile("s_cmp_eq_u32 %0, %1\n\ts_waitcnt lgkmcnt(0)\n\ts_cbranch_scc1 .Lk2mid\n\ts_barrier\n.Lk2mid:"
                   :: "s"(so), "n"(17 * K2_STAGE) : "scc", "memory");
      SBAR();
      APRI(0); TILEA(a, a + 4096);
      APRI(1); TILEB(a + 4096);
      APRI(2); TILEA(a + 4096, an);
    } while (so != 17 * K2_STAGE);
  }
  const unsigned xa = lds0 + wid * 1024 + vl, xb = lds0 + (wid ^ 1) * 1024 + vl;
  f32x4 xr[4];
  auto ldx = [&]() {
#pragma unroll
    for (int i = 0; i < 4; ++i) { xr[i] = *(volatile const LAS f32x4*)(uintptr_t)(xb + i * 8192); SBAR(); }
  };
  APRI(3);
  oa = MFMA32(v1, PHI, oa); PIN(oa); SBAR(); EXP4(sB, 0); PIN(sB); SBAR();
  la = LSUM(P, la); PIN(la); SBAR(); EXP4(sB, 4); PIN(sB); SBAR();
  PK4H(0, sB, 0); PIN(P); SBAR();
  o = MFMA32(v0, PLO, o); PIN(o); SBAR();
  {
#pragma unroll
    for (int i = 1; i < 4; ++i) *(volatile LAS f32x4*)(uintptr_t)(xa + i * 8192) = (f32x4){oa[4 * i], oa[4 * i + 1], oa[4 * i + 2], oa[4 * i + 3]};
    SBAR();
    const unsigned lu = __float_as_uint(la[0] + la[1]);
    const u32x4 t = {__builtin_amdgcn_perm(__float_as_uint(oa[0]), lu, 0x07060501u), __builtin_amdgcn_perm(__float_as_uint(oa[1]), lu, 0x07060502u),
                     __builtin_amdgcn_perm(__float_as_uint(oa[2]), lu, 0x07060503u), __float_as_uint(oa[3]) | 1u};
    *(volatile LAS u32x4*)(uintptr_t)xa = t;
    SBAR();
  }
  EXP4(sB, 8); EXP4(sB, 12); PIN(sB); SBAR();
  ldx();
  PK4H(1, sB, 8); PIN(P); SBAR();
  l = LSUM(P, l);
  PIN(l);
  SBAR();
  o = MFMA32(v1, PHI, o);
  PIN(o);
  SBAR();
#define LQ() lq = l[0] + l[1]
  { SBAR(); SBAR(); }
  const int tw = wid & 3, kh = wid >> 2;
  const unsigned ob = (mt * 4 + tw) * 65536 + kh * 32768;
  constexpr int OD = OP_OD;
  bf16x8 of[OD];
  auto ldo = [&](int j) {
    of[j % OD] = __builtin_bit_cast(bf16x8, __builtin_amdgcn_raw_buffer_load_b128(ors, vl + (j & 3) * 1024, ob + (((j >> 2) + g0) & 7) * 4096, OP_AUX));
  };
  float lq;
  {
    auto miss = [&]() { return __builtin_amdgcn_ballot_w64((__float_as_uint(xr[0][3]) & 1u) == 0u) != 0; };
    if (__builtin_expect(miss(), 0)) {
#pragma unroll 1
      for (int it = 0; it < (1 << 22); ++it) { ldx(); if (!miss()) break; }
    }
    LQ();
    const unsigned t2 = __builtin_amdgcn_perm(__float_as_uint(xr[0][1]), __float_as_uint(xr[0][0]), 0x0c04000cu);
    lq += __uint_as_float(__builtin_amdgcn_perm(__float_as_uint(xr[0][2]), t2, 0x04020100u));
#pragma unroll
    for (int i = 0; i < 16; ++i) o[i] += xr[i >> 2][i & 3];
  }
  if (__builtin_expect(!__all(lq >= LTH && lq < 3.0e38f), 0)) {
    float mx = -3.0e38f;
    const f32x16 zero = (f32x16){};
    const char* kp = kvg + lane * 16;
    for (int kb = 0; kb < 64; ++kb) {
      f32x16 s = MFMA32(*(const bf16x8*)(kp + kb * 4096), q0, zero);
      s = MFMA32(*(const bf16x8*)(kp + kb * 4096 + 1024), q1, s);
#pragma unroll
      for (int r = 0; r < 16; ++r) mx = fmaxf(mx, s[r]);
    }
    mx = xhalf_max(mx);
#pragma unroll
    for (int r = 0; r < 16; ++r) negm[r] = -mx;
    o = (f32x16){};
    l = (f32x4){};
    for (int kb = 0; kb < 64; ++kb) {
      const char* a = kp + kb * 4096;
      f32x16 s = MFMA32(*(const bf16x8*)a, q0, negm);
      s = MFMA32(*(const bf16x8*)(a + 1024), q1, s);
#pragma unroll
      for (int r = 0; r < 16; ++r) s[r] = __builtin_amdgcn_exp2f(s[r]);
      PK4H(0, s, 0); PK4H(1, s, 8);
      o = MFMA32(*(const bf16x8*)(a + 2048), PLO, o);
      o = MFMA32(*(const bf16x8*)(a + 3072), PHI, o);
      l = LSUM(P, l);
    }
    LQ();
  }
  {
    const float inv = __builtin_amdgcn_rcpf(lq);
    const unsigned oo = (tb * 64 + head * 2) * 1024 + vl;
    u32x4 oa = __builtin_bit_cast(u32x4, pack8(o, 0, inv)), ob2 = __builtin_bit_cast(u32x4, pack8(o, 1, inv));
#pragma unroll
    for (int e = 0; e < 4; ++e) { oa[e] = min(oa[e], 0xfffffffeu); ob2[e] = min(ob2[e], 0xfffffffeu); }
    PIN(oa); PIN(ob2);
    __builtin_amdgcn_raw_buffer_store_b128(oa, ors, oo, 0, 16);
    __builtin_amdgcn_raw_buffer_store_b128(ob2, ors, oo + 1024, 0, 16);
    SBAR();
    if (wid >= 4) __builtin_amdgcn_s_setprio(OP_PR);
    KBL(15);
#pragma unroll
    for (int j = 0; j < OP_P0; ++j) { ldo(j); SBAR(); }
  }
  auto nld = [&](int j) { const int a = OP_P0 + 2 * (j + 1), b = j + 1 + OD, c = a < b ? a : b; return j < 0 ? OP_P0 : (c < 32 ? c : 32); };
  const int hi = lane >> 5, q4 = (lane & 15) >> 2;
  const unsigned wl = (hi * 4 + q4) * 128 + ((q4 >> 1) * 64) +
                      ((lane & 1) * 32 + ((lane >> 4) & 1) * 16 + (lane & 2) * 4);
  const unsigned wbE = lds0 + K2_WE + kh * 16384, wbM = lds0 + K2_CV + kh * 32768, wbL = lds0 + (kh + 2) * K2_STAGE;
  unsigned wa0 = wbE + (wl ^ (kh * 64) ^ 64), wa1 = wa0 ^ 64;
  f32x16 acc0 = (f32x16){}, acc1 = (f32x16){};
  bf16x8 w0[3], w1[3];
  auto ldw = [&](int j) {
    if (j == 8) { wa0 += wbM - wbE; wa1 += wbM - wbE; }
    if (j == 24) { wa0 += wbL - wbM; wa1 += wbL - wbM; }
    const unsigned a = (j < 8 ? j : j < 24 ? j - 8 : j - 24) * 2048;
    w0[j % 3] = cat44(lds_tr(wa0 + a), lds_tr(wa0 + a + 1024));
    w1[j % 3] = cat44(lds_tr(wa1 + a), lds_tr(wa1 + a + 1024));
  };
  auto stale = [&](int j) {
    const u32x4 f = __builtin_bit_cast(u32x4, of[j % OD]);
    return __builtin_amdgcn_ballot_w64(max(max(f[0], f[1]), max(f[2], f[3])) == ~0u) != 0;
  };
  auto chk = [&](int j, int n) {
    if (__builtin_expect(stale(j), 0)) {
#pragma unroll 1
      for (int it = 0; it < (1 << 15); ++it) {
        asm volatile("s_sleep %0" :: "n"(PSL) : "memory");
#pragma unroll
        for (int i = j; i < n; ++i) { ldo(i); SBAR(); }
        if (!stale(j)) break;
      }
    }
  };
  const unsigned wfa = lds0 + wid * 1024 + (lane & 3) * 16 + 12;
  unsigned wfl;
  ldw(0); ldw(1);
  chk(0, OP_P0);
  SBAR();
#pragma unroll
  for (int j = 0; j < 32; ++j) {
    if (j == OP_PSW) __builtin_amdgcn_s_setprio(0);
    if (j == 20) { wfl = *(volatile const LAS unsigned*)(uintptr_t)wfa; SBAR(); }
    if (j == 22) {
      SBAR();
      if (__builtin_expect(__builtin_amdgcn_ballot_w64((wfl & 1u) != 0u) != 0, 0)) {
#pragma unroll 1
        for (int it = 0; it < (1 << 22); ++it) {
          wfl = *(volatile const LAS unsigned*)(uintptr_t)wfa;
          if (__builtin_amdgcn_ballot_w64((wfl & 1u) != 0u) == 0) break;
        }
      }
      asm volatile("" ::: "memory");
      SBAR();
    }
    if (j + 2 < 32) ldw(j + 2);
    SBAR();
    if (j) chk(j, nld(j - 1));
    SBAR();
    acc0 = MFMA32(w0[j % 3], of[j % OD], acc0);
    acc1 = MFMA32(w1[j % 3], of[j % OD], acc1);
#pragma unroll
    for (int i = nld(j - 1); i < nld(j); ++i) ldo(i);
    SBAR();
  }
  {
    const f32x16 snd = acc0;
    constexpr int XW = 1024, XC = 8192;
    const unsigned ra = lds0 + wid * XW + vl;
#pragma unroll
    for (int i = 0; i < 4; ++i)
      *(LAS f32x4*)(uintptr_t)(ra + i * XC) = (f32x4){snd[4 * i], snd[4 * i + 1], snd[4 * i + 2], snd[4 * i + 3]};
    const __amdgpu_buffer_rsrc_t brs = __builtin_amdgcn_make_buffer_rsrc(out, 0, 8 << 20, 0x00020000);
    unsigned rb = lds0 + (wid ^ 4) * XW;
    unsigned ta = rb + (lane & 15) * 64 + ((lane >> 4) & 1) * XC + ((hi * 2) ^ ((lane >> 1) & 3)) * 16, tb2 = rb + (lane >> 2) * 64 + ((lane ^ (lane >> 3)) & 3) * 16;
    unsigned od = (lane >> 2) * 4096 + (lane & 3) * 16, os = (mt * 4 + tw) * 32 * 4096 + 2048 + nt * 128 + kh * 64;
    rb += vl;
    asm volatile("" : "+v"(rb), "+v"(ta), "+v"(tb2), "+v"(od), "+s"(os));
    KBL(17);
    f32x4 r[4];
#pragma unroll
    for (int i = 0; i < 4; ++i) r[i] = *(const LAS f32x4*)(uintptr_t)(rb + i * XC);
    SBAR();
#pragma unroll
    for (int i = 0; i < 2; ++i) {
      u32x4 a;
#pragma unroll
      for (int e = 0; e < 4; ++e)
        a[e] = pk2(acc1[8 * i + 2 * e] + r[2 * i + (e >> 1)][2 * (e & 1)], acc1[8 * i + 2 * e + 1] + r[2 * i + (e >> 1)][2 * (e & 1) + 1]);
      *(LAS u32x4*)(uintptr_t)(ta ^ (i * 16)) = a;
    }
    SBAR();
#pragma unroll
    for (int i = 0; i < 2; ++i)
      __builtin_amdgcn_raw_buffer_store_b128(*(const LAS u32x4*)(uintptr_t)(tb2 + i * XC), brs, od, os + i * 65536, B_AUX);
  }
}

extern "C" void kernel_launch(void* const* d_in, const int* in_sizes, int n_in, void* d_out, int out_size,
                              void* d_ws, size_t ws_size, hipStream_t stream) {
  const unsigned short* aq = (const unsigned short*)d_in[0];
  const unsigned short* ak = (const unsigned short*)d_in[1];
  const unsigned short* av = (const unsigned short*)d_in[2];
  const unsigned short* shk = (const unsigned short*)d_in[3];
  const unsigned short* ssk = (const unsigned short*)d_in[4];
  const unsigned short* ssd = (const unsigned short*)d_in[5];
  const unsigned short* x = (const unsigned short*)d_in[6];
  const unsigned short* wqkv = (const unsigned short*)d_in[7];
  const unsigned short* wo = (const unsigned short*)d_in[8];
  char* ws = (char*)d_ws;
  unsigned short* out = (unsigned short*)d_out;
  (void)hipFuncSetAttribute((const void*)k1_qkv, hipFuncAttributeMaxDynamicSharedMemorySize, K1_LDS);
  (void)hipFuncSetAttribute((const void*)k2_attn_conv, hipFuncAttributeMaxDynamicSharedMemorySize, K2_LDS);
  k1_qkv<<<256, 768, K1_LDS, stream>>>(x, wqkv, ak, av, shk, ws);
  k2_attn_conv<<<256, 768, K2_LDS, stream>>>(aq, ssk, ssd, wo, ws, out);
}
```
